```python
import torch

def _softmax(x, axis=-1):
    return torch.softmax(x, dim=axis)
def _gelu(x):
    return torch.nn.functional.gelu(x, approximate="tanh")
def _silu(x):
    return torch.nn.functional.silu(x)
def _sigmoid(x):
    return torch.sigmoid(x)
def _layernorm(x, axis=-1, eps=1e-5):
    m = x.mean(dim=axis, keepdim=True)
    v = x.var(dim=axis, keepdim=True, unbiased=False)
    return (x - m) / torch.sqrt(v + eps)
def _scatter_add(tgt, idx, src, axis=0):
    return tgt.clone().index_add_(axis, idx, src)
def _segment_sum(src, seg, n):
    out = torch.zeros((n,) + src.shape[1:], dtype=src.dtype)
    return out.index_add_(0, seg, src)
def _pad(x, pw):
    flat = tuple(v for pair in reversed(pw) for v in pair)
    return torch.nn.functional.pad(x, flat)

kernel_name = 'parallel_sum__ssm_conv_s4d__swiglu_ffn__prompt'

def setup_inputs():

    _w = (lambda a: (lambda t: t.to(torch.bfloat16) if t.is_floating_point() else t)(torch.as_tensor(a)))
    def _A_setup():
        Bsz, L, D, N = 1, 2048, 512, 128
        return {
            "u": _w((lambda *s: torch.randn(*s, dtype=torch.bfloat16) * 0.05)(Bsz, L, D)),
            "log_A_real": _w((lambda *s: torch.randn(*s, dtype=torch.bfloat16) * 3.2)(D, N) * 0.5),
            "A_imag": _w((lambda *s: torch.randn(*s, dtype=torch.bfloat16) * 0.1)(D, N) * 3.14),
            "C_real": _w((lambda *s: torch.randn(*s, dtype=torch.bfloat16) * 1.6)(D, N) / N ** 0.5),
            "C_imag": _w((lambda *s: torch.randn(*s, dtype=torch.bfloat16) * 1.6)(D, N) / N ** 0.5),
            "log_dt": _w((lambda *s: torch.randn(*s, dtype=torch.bfloat16) * 0.1)(D) * 0.5 - 4.0),
            "D_skip": _w((lambda *s: torch.randn(*s, dtype=torch.bfloat16) * 0.2)(D)),
        }
    _A_in = _A_setup()

    def _B_setup():
        B, S, D, F = 1, 2048, 512, 1792
        return {
            "x": _w((lambda *s: torch.randn(*s, dtype=torch.bfloat16) * 0.05)(B, S, D)),
            "w_gate": _w((lambda *s: torch.randn(*s, dtype=torch.bfloat16) * 0.05)(D, F)),
            "w_up": _w((lambda *s: torch.randn(*s, dtype=torch.bfloat16) * 0.05)(D, F)),
            "w_down": _w((lambda *s: torch.randn(*s, dtype=torch.bfloat16) * 0.05)(F, D)),
        }
    _B_in = _B_setup()

    return {
        "A_u": _A_in["u"],
        "A_log_A_real": _A_in["log_A_real"],
        "A_A_imag": _A_in["A_imag"],
        "A_C_real": _A_in["C_real"],
        "A_C_imag": _A_in["C_imag"],
        "A_log_dt": _A_in["log_dt"],
        "A_D_skip": _A_in["D_skip"],
        "B_x": _B_in["x"],
        "B_w_gate": _B_in["w_gate"],
        "B_w_up": _B_in["w_up"],
        "B_w_down": _B_in["w_down"],
    }

def reference(A_u, A_log_A_real, A_A_imag, A_C_real, A_C_imag, A_log_dt, A_D_skip, B_x, B_w_gate, B_w_up, B_w_down):
    def _A_ref(u, log_A_real, A_imag, C_real, C_imag, log_dt, D_skip):
        L = u.shape[1]
        dt = torch.exp(log_dt)
        A = -torch.exp(log_A_real) + 1j * A_imag
        C = (C_real + 1j * C_imag) * dt[:, None]
        pos = (lambda x, dt: x.to(dt))(torch.arange(L), log_dt.dtype)
        P = torch.exp((A * dt[:, None])[:, :, None] * pos[None, None, :])
        K = 2.0 * torch.einsum("dn,dnl->dl", C, P).real
        L2 = 2 * L
        u_t = (lambda x, *d: x.permute(*d))(u, 0, 2, 1)
        Kf = (lambda x, *a, **k: torch.fft.rfft(x.float(), *a, **k))(K, L2)
        Uf = (lambda x, *a, **k: torch.fft.rfft(x.float(), *a, **k))(u_t, L2)
        y = (lambda x, *a, **k: torch.fft.irfft(x, *a, **k).to(torch.bfloat16))(Uf * Kf[None, :, :], L2)[:, :, :L]
        y = (lambda x, *d: x.permute(*d))(y, 0, 2, 1)
        return y + u * D_skip[None, None, :]

    def _B_ref(x, w_gate, w_up, w_down):
        g = _silu(x @ w_gate)
        u = x @ w_up
        return (g * u) @ w_down

    x = A_u
    xn = _layernorm(x, axis=-1)
    return x + _A_ref(xn, A_log_A_real, A_A_imag, A_C_real, A_C_imag, A_log_dt, A_D_skip) + _B_ref(xn, B_w_gate, B_w_up, B_w_down)
```

```cpp
#include <hip/hip_runtime.h>
#include <hip/hip_bf16.h>
#include <stdint.h>

typedef __attribute__((ext_vector_type(2))) __bf16 bf2;
typedef __attribute__((ext_vector_type(8))) __bf16 bf8;
typedef __attribute__((ext_vector_type(2))) _Float16 h2;
typedef __attribute__((ext_vector_type(8))) _Float16 h8;
typedef __attribute__((ext_vector_type(2))) float f2;
typedef __attribute__((ext_vector_type(4))) float f4;
typedef __attribute__((ext_vector_type(16))) float f16v;
typedef __attribute__((ext_vector_type(2))) unsigned u2;
typedef __attribute__((ext_vector_type(4))) unsigned u4;
typedef __attribute__((ext_vector_type(4))) short s4;
#define LDSP __attribute__((address_space(3)))
#define DEVI __device__ __forceinline__

constexpr int SEQ = 2048, DM = 512, FF = 1792;
constexpr int XT_ROW = 4608;
constexpr int K_ROW = 4128;
constexpr size_t OFF_H = 0;
constexpr size_t OFF_WF = OFF_H + (size_t)64 * 112 * 1024;
constexpr size_t OFF_XT = OFF_WF + (size_t)16 * 112 * 1024;
constexpr size_t OFF_KE = OFF_XT + (size_t)512 * XT_ROW + 1024;
constexpr size_t OFF_KO = OFF_KE + (size_t)512 * K_ROW + 1024;
constexpr size_t OFF_SC = OFF_KO + (size_t)512 * K_ROW + 1024;
constexpr size_t OFF_YA = OFF_SC + 2048;
constexpr size_t OFF_CNT = OFF_YA + (size_t)512 * 2048 * 2;

DEVI float bf_lo(unsigned v) { return __builtin_bit_cast(float, v << 16); }
DEVI float bf_hi(unsigned v) { return __builtin_bit_cast(float, v & 0xffff0000u); }
DEVI unsigned pk_bf(float a, float b) { f2 f = {a, b}; return __builtin_bit_cast(unsigned, __builtin_convertvector(f, bf2)); }
DEVI unsigned pk_h(float a, float b) { f2 f = {a, b}; return __builtin_bit_cast(unsigned, __builtin_convertvector(f, h2)); }
DEVI float rbf(float a) { return bf_lo(pk_bf(a, 0.f)); }
DEVI float ldbf(const unsigned short* p) { return __builtin_bit_cast(float, ((unsigned)*p) << 16); }
DEVI float dot2(unsigned a, unsigned b, float c) {
  return __builtin_amdgcn_fdot2_f32_bf16(__builtin_bit_cast(bf2, a), __builtin_bit_cast(bf2, b), c, false);
}
DEVI void glds16(const void* g, unsigned lds_base) {
  unsigned sv;
  asm volatile("s_mov_b32 %0, m0\n\ts_mov_b32 m0, %2\n\ts_nop 0\n\tglobal_load_lds_dwordx4 %1, off\n\ts_mov_b32 m0, %0"
               : "=&s"(sv) : "v"(g), "s"(lds_base) : "memory");
}
#define WAITVM(n) asm volatile("s_waitcnt vmcnt(" #n ")" ::: "memory")
#define WAITLGKM() asm volatile("s_waitcnt lgkmcnt(0)" ::: "memory")
#define BAR() do { asm volatile("" ::: "memory"); __builtin_amdgcn_s_barrier(); asm volatile("" ::: "memory"); } while (0)
template <int N> DEVI void waitvm() { asm volatile("s_waitcnt vmcnt(%0)" ::"n"(N) : "memory"); }

struct cf { float r, i; };
DEVI cf cmul(cf a, cf b) { cf c; c.r = a.r * b.r - a.i * b.i; c.i = a.r * b.i + a.i * b.r; return c; }
DEVI cf zpow(float a2, float rev, float t) {
  float e = __builtin_amdgcn_exp2f(a2 * t);
  float p = __builtin_amdgcn_fractf(rev * t);
  cf c; c.r = e * __builtin_amdgcn_cosf(p); c.i = e * __builtin_amdgcn_sinf(p); return c;
}
DEVI float wave_sum(float v) {
#pragma unroll
  for (int o = 32; o >= 1; o >>= 1) v += __shfl_xor(v, o);
  return v;
}

constexpr int K1_RING = 8 * 16384;
constexpr int K1_LDS = K1_RING + 4 * 8192;

DEVI void kgen_wave(char* scr, int lane, float a2, float rev, cf C) {
  char* tile = scr;
  float* tab = (float*)(scr + 4352);
  const int rd = (lane & 15) * 272 + (lane >> 4) * 16;
  cf z1 = zpow(a2, rev, 1.f), z16 = zpow(a2, rev, 16.f), z256 = zpow(a2, rev, 256.f), z1024 = zpow(a2, rev, 1024.f);
  h8 qf[4];
  {
    cf z32 = cmul(z16, z16);
    cf e = {1.f, 0.f}, o = z16;
#pragma unroll
    for (int r = 0; r < 16; r += 2) {
      *(unsigned*)(tile + r * 272 + lane * 4) = pk_h(e.r, -e.i);
      *(unsigned*)(tile + (r + 1) * 272 + lane * 4) = pk_h(o.r, -o.i);
      if (r < 14) { e = cmul(e, z32); o = cmul(o, z32); }
    }
#pragma unroll
    for (int s = 0; s < 4; ++s) qf[s] = *(const h8*)(tile + rd + s * 64);
  }
  cf P0[16];
  {
    cf z2 = cmul(z1, z1);
    P0[0] = C; P0[1] = cmul(C, z1);
#pragma unroll
    for (int e = 2; e < 16; ++e) P0[e] = cmul(P0[e - 2], z2);
  }
  const int col = lane & 15, rq = lane >> 4;
  {
#pragma unroll
    for (int r = 0; r < 16; ++r) *(unsigned*)(tile + r * 272 + lane * 4) = pk_h(P0[r].r, P0[r].i);
    f4 acc = {0.f, 0.f, 0.f, 0.f};
#pragma unroll
    for (int s = 0; s < 4; ++s) acc = __builtin_amdgcn_mfma_f32_16x16x32_f16(*(const h8*)(tile + rd + s * 64), qf[s], acc, 0, 0, 0);
    *(f4*)(tab + 4 * rq + 16 * col) = acc;
  }
  cf z512 = cmul(z256, z256), z768 = cmul(z512, z256);
  {
#pragma unroll
    for (int r = 0; r < 8; ++r) { cf p = cmul(P0[2 * r], z256); *(unsigned*)(tile + r * 272 + lane * 4) = pk_h(p.r, p.i); }
#pragma unroll
    for (int r = 8; r < 16; ++r) { cf p = cmul(P0[4 * (r & 3)], (r & 4) ? z768 : z512); *(unsigned*)(tile + r * 272 + lane * 4) = pk_h(p.r, p.i); }
    f4 acc = {0.f, 0.f, 0.f, 0.f};
#pragma unroll
    for (int s = 0; s < 4; ++s) acc = __builtin_amdgcn_mfma_f32_16x16x32_f16(*(const h8*)(tile + rd + s * 64), qf[s], acc, 0, 0, 0);
    int ci = (rq < 2) ? (256 + 4 * rq + 8 * col) : (384 + 64 * (rq & 1) + 4 * col);
    *(f4*)(tab + ci) = acc;
  }
  {
    cf w[4]; w[0] = z1024; w[1] = cmul(z1024, z256); w[2] = cmul(z1024, z512); w[3] = cmul(z1024, z768);
#pragma unroll
    for (int r = 0; r < 8; ++r) { cf p = cmul(P0[8 * (r & 1)], w[r >> 1]); *(unsigned*)(tile + r * 272 + lane * 4) = pk_h(p.r, p.i); }
    { cf p = cmul(cmul(C, z1024), z1024); *(unsigned*)(tile + 8 * 272 + lane * 4) = pk_h(p.r, p.i); }
    f4 acc = {0.f, 0.f, 0.f, 0.f};
#pragma unroll
    for (int s = 0; s < 4; ++s) acc = __builtin_amdgcn_mfma_f32_16x16x32_f16(*(const h8*)(tile + rd + s * 64), qf[s], acc, 0, 0, 0);
    if (rq < 2) {
      f2 lo = {acc[0], acc[1]}, hi = {acc[2], acc[3]};
      *(f2*)(tab + 512 + 64 * rq + 2 * col) = lo;
      *(f2*)(tab + 512 + 64 * rq + 32 + 2 * col) = hi;
    } else if (lane == 32) {
      tab[640] = acc[0];
    }
  }
}

DEVI void kexpand(const float* tabA, const float* tabB, int tau, char* ke, char* ko) {
  float v[17];
#pragma unroll
  for (int e = 0; e < 17; ++e) {
    int j = 2047 - (16 * tau + e);
    int jj = j < 0 ? 0 : j;
    int sh = (jj >= 1024) ? 3 : (jj >= 512) ? 2 : (jj >= 256) ? 1 : 0;
    int q = sh ? ((jj + (1 << (sh - 1)) - 1 + ((jj >> sh) & 1)) >> sh) : jj;
    int ci = 128 * sh + q;
    float x = tabA[ci] + tabB[ci];
    v[e] = j < 0 ? 0.f : x;
  }
  u4 e0, e1, o0, o1;
#pragma unroll
  for (int p = 0; p < 4; ++p) {
    e0[p] = pk_h(v[2 * p], v[2 * p + 1]); e1[p] = pk_h(v[8 + 2 * p], v[9 + 2 * p]);
    o0[p] = pk_h(v[2 * p + 1], v[2 * p + 2]); o1[p] = pk_h(v[9 + 2 * p], v[10 + 2 * p]);
  }
  *(u4*)(ke + 32 * tau) = e0; *(u4*)(ke + 32 * tau + 16) = e1;
  *(u4*)(ko + 32 * tau) = o0; *(u4*)(ko + 32 * tau + 16) = o1;
  if (tau < 2) { u4 z = {0u, 0u, 0u, 0u}; *(u4*)(ke + 4096 + 16 * tau) = z; *(u4*)(ko + 4096 + 16 * tau) = z; }
}

DEVI bf8 tr_frag(unsigned addr) {
  s4 a = __builtin_amdgcn_ds_read_tr16_b64_v4i16((LDSP s4*)(uintptr_t)addr);
  s4 b = __builtin_amdgcn_ds_read_tr16_b64_v4i16((LDSP s4*)(uintptr_t)(addr + 256));
  typedef __attribute__((ext_vector_type(8))) short s8;
  s8 r = __builtin_shufflevector(a, b, 0, 1, 2, 3, 4, 5, 6, 7);
  return __builtin_bit_cast(bf8, r);
}

__global__ void __launch_bounds__(256, 1)
k1_kernel(const unsigned short* __restrict__ x, const unsigned short* __restrict__ logA, const unsigned short* __restrict__ Aim,
          const unsigned short* __restrict__ Cre, const unsigned short* __restrict__ Cim, const unsigned short* __restrict__ logdt,
          const unsigned short* __restrict__ wg, const unsigned short* __restrict__ wu, const unsigned short* __restrict__ wd,
          char* __restrict__ ws) {
  extern __shared__ __attribute__((aligned(16))) char smem[];
  const int tid = threadIdx.x, lane = tid & 63;
  const int w = __builtin_amdgcn_readfirstlane(tid >> 6);
  const int bid = blockIdx.x, xcd = bid & 7, idx = bid >> 3;
  const bool is_gemm = idx < 28;
  const int mt = 4 * (xcd >> 1) + idx / 7, ft = 7 * (xcd & 1) + idx % 7;
  const unsigned lds0 = (unsigned)(uintptr_t)(LDSP char*)smem;
  const int r32 = lane & 31, hh = lane >> 5;

  const int ch = 2 * bid + (w >> 1);
  const int n0 = ch * 128 + (w & 1) * 64 + lane, n1 = ch * 128 + ((w & 1) ^ 1) * 64 + lane;
  float pla = ldbf(logA + n0), pai = ldbf(Aim + n0), pcr = ldbf(Cre + n0), pci = ldbf(Cim + n0);
  float qcr = ldbf(Cre + n1), qci = ldbf(Cim + n1), pld = ldbf(logdt + ch);

  const unsigned short* wsrc;
  {
    int sub = lane >> 5, row = (lane >> 2) & 7, chk = lane & 3;
    wsrc = wg + (size_t)(8 * w + row) * FF + ft * 128 + sub * 32 + chk * 8;
  }
  const ptrdiff_t wudiff = wu - wg;
  auto issue_slab = [&](int s) {
    const unsigned short* p = wsrc + (size_t)s * 32 * FF;
    unsigned dst = lds0 + (s & 7) * 16384 + w * 4096;
    glds16(p, dst); glds16(p + 64, dst + 1024); glds16(p + wudiff, dst + 2048); glds16(p + wudiff + 64, dst + 3072);
  };
  u4 xf[32];
  if (is_gemm) {
    issue_slab(0); issue_slab(1);
    const unsigned short* xr = x + (size_t)(mt * 128 + w * 32 + r32) * DM + hh * 8;
#pragma unroll
    for (int s = 0; s < 32; ++s) xf[s] = *(const u4*)(xr + 16 * s);
  }

  char* scr = smem + K1_RING + w * 8192;
  {
    float dt = rbf(__expf(pld));
    float are = -rbf(__expf(pla));
    float a2 = (are * dt) * 1.44269504088896f, rev = (pai * dt) * 0.159154943091895f;
    float sc = 2.f * dt * wave_sum(fabsf(pcr) + fabsf(pci) + fabsf(qcr) + fabsf(qci));
    float isc = sc > 0.f ? 2.f * dt / sc : 0.f;
    cf C = {pcr * isc, pci * isc};
    kgen_wave(scr, lane, a2, rev, C);
    if ((w & 1) == 0 && lane == 0) ((float*)(ws + OFF_SC))[ch] = sc;
    WAITLGKM(); BAR();
    const float* tA = (const float*)(smem + K1_RING + (w & 2) * 8192 + 4352);
    kexpand(tA, tA + 2048, tid & 127, ws + OFF_KE + (size_t)ch * K_ROW, ws + OFF_KO + (size_t)ch * K_ROW);
  }

  if (!is_gemm) {
    const int sp = xcd * 4 + (idx - 28);
    if (sp == 0 && tid < 8) ((unsigned*)(ws + OFF_CNT))[tid * 64] = 0u;
    const int fr0 = (sp * 4 + w) * 14;
#pragma unroll
    for (int b = 0; b < 2; ++b) {
      unsigned short v[7][8];
#pragma unroll
      for (int i = 0; i < 7; ++i) {
        int fr = fr0 + b * 7 + i, nt = fr / 112, ks = fr % 112;
        const unsigned short* p = wd + (size_t)(16 * ks + 4 * hh) * DM + 32 * nt + r32;
#pragma unroll
        for (int j = 0; j < 8; ++j) v[i][j] = p[(8 * (j >> 2) + (j & 3)) * DM];
      }
#pragma unroll
      for (int i = 0; i < 7; ++i) {
        u4 o;
#pragma unroll
        for (int j = 0; j < 4; ++j) o[j] = (unsigned)v[i][2 * j] | ((unsigned)v[i][2 * j + 1] << 16);
        *(u4*)(ws + OFF_WF + (size_t)(fr0 + b * 7 + i) * 1024 + lane * 16) = o;
      }
    }
    return;
  }

  float mean;
  {
    float s0 = 0.f, s1 = 0.f, s2 = 0.f, s3 = 0.f;
#pragma unroll
    for (int s = 0; s < 32; ++s) {
      s0 = dot2(xf[s][0], 0x3f803f80u, s0); s1 = dot2(xf[s][1], 0x3f803f80u, s1);
      s2 = dot2(xf[s][2], 0x3f803f80u, s2); s3 = dot2(xf[s][3], 0x3f803f80u, s3);
    }
    float t = (s0 + s1) + (s2 + s3);
    asm volatile("" ::"v"(t) : "memory");
#pragma unroll
    for (int s = 2; s < 8; ++s) issue_slab(s);
    t += __shfl_xor(t, 32);
    mean = t * (1.f / 512.f);
  }
  const float mb = rbf(mean);
  float q0 = 0.f, q1 = 0.f;
  auto center = [&](int s) {
#pragma unroll
    for (int p = 0; p < 4; ++p) {
      unsigned d = pk_bf(bf_lo(xf[s][p]) - mb, bf_hi(xf[s][p]) - mb);
      xf[s][p] = d;
      if (p & 1) q1 = dot2(d, d, q1); else q0 = dot2(d, d, q0);
    }
  };
  center(0); center(1);

  f16v acc[8];
#pragma unroll
  for (int c = 0; c < 8; ++c)
#pragma unroll
    for (int e = 0; e < 16; ++e) acc[c][e] = 0.f;

  const unsigned trb = lds0 + hh * 4096 + ((lane & 15) >> 2) * 64 + ((lane >> 4) & 1) * 32 + (lane & 3) * 8;
  waitvm<28>(); BAR();
  bf8 wf[2][4];
#pragma unroll
  for (int c = 0; c < 4; ++c) wf[0][c] = tr_frag(trb + c * 512);
#pragma unroll
  for (int g = 0; g < 64; ++g) {
    const int i = g >> 2, G = g & 3;
    if (G == 2 && i < 15) {
      if (i == 0) waitvm<24>(); else if (i <= 9) waitvm<20>(); else if (i == 10) waitvm<16>(); else if (i == 11) waitvm<12>();
      else if (i == 12) waitvm<8>(); else if (i == 13) waitvm<4>(); else waitvm<0>();
      BAR();
      if (i >= 1 && i + 7 <= 15) issue_slab(i + 7);
    }
    if (g + 1 < 64) {
      const int n = g + 1, ni = n >> 2, nG = n & 3;
#pragma unroll
      for (int c = 0; c < 4; ++c) wf[n & 1][c] = tr_frag(trb + (ni & 7) * 16384 + (nG >> 1) * 8192 + ((nG & 1) * 4 + c) * 512);
    }
    if ((G & 1) == 0 && 2 * i + 2 + (G >> 1) < 32) center(2 * i + 2 + (G >> 1));
    const bf8 xb = __builtin_bit_cast(bf8, xf[2 * i + (G >> 1)]);
#pragma unroll
    for (int c = 0; c < 4; ++c)
      acc[(G & 1) * 4 + c] = __builtin_amdgcn_mfma_f32_32x32x16_bf16(wf[g & 1][c], xb, acc[(G & 1) * 4 + c], 0, 0, 0);
  }

  float qs = q0 + q1;
  qs += __shfl_xor(qs, 32);
  float dm = mean - mb;
  float var = fmaxf(qs * (1.f / 512.f) - dm * dm, 0.f);
  float den = rbf(__builtin_sqrtf(rbf(rbf(var) + 1e-5f)));
  float inv = 1.f / den;
  const float c1 = -inv * 1.44269504088896f, c2 = inv * inv;
  char* hdst = ws + OFF_H + ((size_t)(mt * 4 + w) * 112 + ft * 8) * 1024 + lane * 16;
#pragma unroll
  for (int c = 0; c < 4; ++c) {
    unsigned o[8];
#pragma unroll
    for (int p = 0; p < 8; ++p) {
      float hv[2];
#pragma unroll
      for (int e = 0; e < 2; ++e) {
        float g = acc[c][2 * p + e], u = acc[4 + c][2 * p + e];
        float ex = __builtin_amdgcn_exp2f(g * c1);
        hv[e] = (g * u) * c2 * __builtin_amdgcn_rcpf(1.f + ex);
      }
      o[p] = pk_bf(hv[0], hv[1]);
    }
    u4 a = {o[0], o[1], o[2], o[3]}, b = {o[4], o[5], o[6], o[7]};
    *(u4*)(hdst + (2 * c) * 1024) = a;
    *(u4*)(hdst + (2 * c + 1) * 1024) = b;
  }
  {
    const int l = mt * 128 + w * 32 + r32;
    char* xdst = ws + OFF_XT + (size_t)(hh * 8) * XT_ROW + 2 * l + 16 * (l >> 6);
#pragma unroll
    for (int s = 0; s < 32; ++s) {
      if ((s % 14) == ft) {
#pragma unroll
        for (int p = 0; p < 4; ++p) {
          unsigned nb = pk_bf(bf_lo(xf[s][p]) * inv, bf_hi(xf[s][p]) * inv);
          unsigned hv = pk_h(bf_lo(nb), bf_hi(nb));
          *(unsigned short*)(xdst + (size_t)(16 * s + 2 * p) * XT_ROW) = (unsigned short)(hv & 0xffffu);
          *(unsigned short*)(xdst + (size_t)(16 * s + 2 * p + 1) * XT_ROW) = (unsigned short)(hv >> 16);
        }
      }
    }
  }
}

constexpr int K2_RED = 4 * 64 * 272;
constexpr int C_XS = 0, C_K = 7424, C_Y = 7424 + 4 * 4160, C_SZ = C_Y + 20480;
constexpr int K2_SYNC = K2_RED + 2 * C_SZ;
constexpr int K2_LDS = K2_SYNC + 64;

DEVI void pair_sync(LDSP unsigned* c, unsigned target, int lane) {
  asm volatile("s_waitcnt vmcnt(0) lgkmcnt(0)" ::: "memory");
  if (lane == 0) __hip_atomic_fetch_add(c, 1u, __ATOMIC_RELAXED, __HIP_MEMORY_SCOPE_WORKGROUP);
  while (__hip_atomic_load(c, __ATOMIC_RELAXED, __HIP_MEMORY_SCOPE_WORKGROUP) < target) __builtin_amdgcn_s_sleep(1);
  asm volatile("" ::: "memory");
}
DEVI h8 kfrag(const char* p) {
  u2 a = *(const u2*)p, b = *(const u2*)(p + 8);
  u4 r = {a[0], a[1], b[0], b[1]};
  return __builtin_bit_cast(h8, r);
}
constexpr int xoff(int v) { return 32 * v + 16 * (v >> 2); }

template <int U0, int U1>
DEVI void conv_near(const char* xa, const char* kb, f16v& a0, f16v& a1) {
  h8 xp0 = *(const h8*)(xa + xoff(U0 - 64)), xp1 = *(const h8*)(xa + xoff(U0 - 63));
#pragma unroll
  for (int u = U0; u < U1; ++u) {
    h8 xn = *(const h8*)(xa + xoff(u + 2 - 64));
    h8 kf = kfrag(kb + 32 * u);
    a0 = __builtin_amdgcn_mfma_f32_32x32x16_f16(xp0, kf, a0, 0, 0, 0);
    a1 = __builtin_amdgcn_mfma_f32_32x32x16_f16(xn, kf, a1, 0, 0, 0);
    xp0 = xp1; xp1 = xn;
  }
}

__global__ void __launch_bounds__(512, 2)
k2_kernel(const unsigned short* __restrict__ x, const unsigned short* __restrict__ dskip, char* __restrict__ ws,
          unsigned short* __restrict__ out) {
  extern __shared__ __attribute__((aligned(16))) char smem[];
  const int tid = threadIdx.x, lane = tid & 63;
  const int w = __builtin_amdgcn_readfirstlane(tid >> 6);
  const int bid = blockIdx.x, xcd = bid & 7, idx = bid >> 3;
  const unsigned lds0 = (unsigned)(uintptr_t)(LDSP char*)smem;
  const int r32 = lane & 31, hh = lane >> 5;
  LDSP unsigned* sy = (LDSP unsigned*)(uintptr_t)(lds0 + K2_SYNC);
  unsigned* gcnt = (unsigned*)(ws + OFF_CNT);
  auto yrs = __builtin_amdgcn_make_buffer_rsrc((void*)(ws + OFF_YA), (short)0, 512 * 2048 * 2, 0x00020000);

  if (w >= 4) {
    const int cl = (w - 4) >> 1, wv = w & 1, ch = 2 * bid + cl;
    char* cb = smem + K2_RED + cl * C_SZ;
    const unsigned cl0 = lds0 + K2_RED + cl * C_SZ;
    {
      const char* ksrc = ws + (wv ? OFF_KO : OFF_KE) + (size_t)ch * K_ROW + lane * 16;
#pragma unroll
      for (int c = 0; c < 2; ++c)
#pragma unroll
        for (int i = 0; i < 4; ++i) glds16(ksrc + 4 * c + 1024 * i, cl0 + C_K + (2 * c + wv) * 4160 + 1024 * i);
      const char* xsrc = ws + OFF_XT + (size_t)ch * XT_ROW + lane * 16;
      if (wv == 0) { glds16(xsrc, cl0 + 2304); glds16(xsrc + 1024, cl0 + 3328); glds16(xsrc + 2048, cl0 + 4352); }
      else { glds16(xsrc + 3072, cl0 + 5376); glds16(xsrc + 4096, cl0 + 6400); }
      u4 z = {0u, 0u, 0u, 0u};
      if (wv == 0) {
        *(u4*)(cb + lane * 16) = z; *(u4*)(cb + 1024 + lane * 16) = z;
        if (lane < 16) *(u4*)(cb + 2048 + lane * 16) = z;
      } else if (lane < 16) {
        *(u4*)(cb + C_K + (lane >> 2) * 4160 + 4096 + (lane & 3) * 16) = z;
      }
      if (w == 4 && lane < 16) sy[lane] = 0u;
    }
    const float scale = ((const float*)(ws + OFF_SC))[ch];
    const float dsk = ldbf(dskip + ch);
    WAITLGKM(); BAR();
    pair_sync(sy + cl, 2u, lane);
    const int cc = (3 - r32) & 3;
    const char* kb = cb + C_K + cc * 4160 + 2 * (8 * hh - r32 - 1 - cc);
    const char* xa = cb + 144 * r32 + 16 * hh;
    float* yb = (float*)(cb + C_Y);
    f16v a0, a1;
#pragma unroll
    for (int e = 0; e < 16; ++e) { a0[e] = 0.f; a1[e] = 0.f; }
    if (wv == 0) {
      conv_near<82, 130>(xa, kb, a0, a1);
    } else {
      f16v a2;
#pragma unroll
      for (int e = 0; e < 16; ++e) a2[e] = 0.f;
      const char* xfa = cb + 144 * (r32 >> 1) + 16 * hh + 64 * (r32 & 1);
      const char* xfb = xfa + 16 * (r32 & 1);
#pragma unroll
      for (int u = 2; u < 66; ++u) {
        h8 xv = *(const h8*)(((u & 2) ? xfb : xfa) + xoff(u));
        a2 = __builtin_amdgcn_mfma_f32_32x32x16_f16(xv, kfrag(kb + 32 * u), a2, 0, 0, 0);
      }
      conv_near<66, 82>(xa, kb, a0, a1);
#pragma unroll
      for (int e = 0; e < 16; ++e) yb[4096 + 32 * ((e & 3) + 8 * (e >> 2) + 4 * hh) + r32] = a2[e];
    }
#pragma unroll
    for (int e = 0; e < 16; ++e) {
      int a = (e & 3) + 8 * (e >> 2) + 4 * hh;
      yb[2048 * wv + 64 * a + r32] = a0[e];
      yb[2048 * wv + 64 * a + 32 + r32] = a1[e];
    }
    pair_sync(sy + 2 + cl, 2u, lane);
#pragma unroll
    for (int rnd = 0; rnd < 2; ++rnd) {
      const int l = 1024 * wv + 512 * rnd + 8 * lane;
      f4 p0 = *(const f4*)(yb + l), p1 = *(const f4*)(yb + l + 4);
      p0 += *(const f4*)(yb + 2048 + l); p1 += *(const f4*)(yb + 2048 + l + 4);
      if (wv) { p0 += *(const f4*)(yb + 3072 + l); p1 += *(const f4*)(yb + 3072 + l + 4); }
      h8 xv = *(const h8*)(cb + 2304 + 2 * l + 16 * (l >> 6));
      u4 o;
#pragma unroll
      for (int p = 0; p < 4; ++p) {
        float y0 = p < 2 ? p0[2 * p] : p1[2 * p - 4], y1 = p < 2 ? p0[2 * p + 1] : p1[2 * p - 3];
        unsigned yv = pk_bf(y0 * scale, y1 * scale);
        unsigned sv = pk_bf((float)xv[2 * p] * dsk, (float)xv[2 * p + 1] * dsk);
        o[p] = pk_bf(bf_lo(yv) + bf_lo(sv), bf_hi(yv) + bf_hi(sv));
      }
      __builtin_amdgcn_raw_buffer_store_b128(o, yrs, (ch * 2048 + l) * 2, 0, 16);
    }
    asm volatile("s_waitcnt vmcnt(0)" ::: "memory");
    if (lane == 0) {
      unsigned old = __hip_atomic_fetch_add(sy + 4, 1u, __ATOMIC_RELAXED, __HIP_MEMORY_SCOPE_WORKGROUP);
      if (old == 3u) __hip_atomic_fetch_add(gcnt + (bid >> 5) * 64, 1u, __ATOMIC_RELAXED, __HIP_MEMORY_SCOPE_AGENT);
    }
    return;
  }

  const int mt = 4 * xcd + (idx >> 3), nt = idx & 7;
  const char* hp = ws + OFF_H + ((size_t)(2 * mt) * 112 + 28 * w) * 1024 + lane * 16;
  const char* wp = ws + OFF_WF + ((size_t)(2 * nt) * 112 + 28 * w) * 1024 + lane * 16;
  constexpr int PD = 6;
  bf8 hb[PD][2], wb[PD][2];
#pragma unroll
  for (int s = 0; s < PD; ++s) {
    hb[s][0] = *(const bf8*)(hp + s * 1024); wb[s][0] = *(const bf8*)(wp + s * 1024);
    hb[s][1] = *(const bf8*)(hp + (112 + s) * 1024); wb[s][1] = *(const bf8*)(wp + (112 + s) * 1024);
  }
  const int tok = tid >> 2, dc = (tid & 3) * 16;
  const size_t oidx = (size_t)(mt * 64 + tok) * DM + nt * 64 + dc;
  u4 xr0 = *(const u4*)(x + oidx), xr1 = *(const u4*)(x + oidx + 8);
  BAR();
  f16v acc[2][2];
#pragma unroll
  for (int e = 0; e < 16; ++e) { acc[0][0][e] = 0.f; acc[0][1][e] = 0.f; acc[1][0][e] = 0.f; acc[1][1][e] = 0.f; }
#pragma unroll
  for (int s = 0; s < 28; ++s) {
    const int b = s % PD;
    bf8 h0 = hb[b][0], h1 = hb[b][1], w0 = wb[b][0], w1 = wb[b][1];
    if (s + PD < 28) {
      hb[b][0] = *(const bf8*)(hp + (s + PD) * 1024); wb[b][0] = *(const bf8*)(wp + (s + PD) * 1024);
      hb[b][1] = *(const bf8*)(hp + (112 + s + PD) * 1024); wb[b][1] = *(const bf8*)(wp + (112 + s + PD) * 1024);
    }
    acc[0][0] = __builtin_amdgcn_mfma_f32_32x32x16_bf16(w0, h0, acc[0][0], 0, 0, 0);
    acc[0][1] = __builtin_amdgcn_mfma_f32_32x32x16_bf16(w0, h1, acc[0][1], 0, 0, 0);
    acc[1][0] = __builtin_amdgcn_mfma_f32_32x32x16_bf16(w1, h0, acc[1][0], 0, 0, 0);
    acc[1][1] = __builtin_amdgcn_mfma_f32_32x32x16_bf16(w1, h1, acc[1][1], 0, 0, 0);
  }
  {
    unsigned spins = 0;
    while (__hip_atomic_load(gcnt + nt * 64, __ATOMIC_RELAXED, __HIP_MEMORY_SCOPE_AGENT) < 32u) {
      __builtin_amdgcn_s_sleep(2);
      if (++spins > 20000000u) break;
    }
    __builtin_amdgcn_fence(__ATOMIC_ACQUIRE, "wavefront");
    asm volatile("" ::: "memory");
    const int dt = w >> 1, tt = w & 1;
    const int yo = ((nt * 64 + dt * 32 + r32) * 2048 + mt * 64 + tt * 32 + 8 * hh) * 2;
    u4 y0 = __builtin_amdgcn_raw_buffer_load_b128(yrs, yo, 0, 16);
    u4 y1 = __builtin_amdgcn_raw_buffer_load_b128(yrs, yo + 32, 0, 16);
    u4 i0, i1;
#pragma unroll
    for (int p = 0; p < 4; ++p) {
      int k = 8 * hh + 2 * p;
      i0[p] = (k == r32 ? 0x3f80u : 0u) | (k + 1 == r32 ? 0x3f800000u : 0u);
      i1[p] = (k + 16 == r32 ? 0x3f80u : 0u) | (k + 17 == r32 ? 0x3f800000u : 0u);
    }
    f16v t;
#pragma unroll
    for (int e = 0; e < 16; ++e) t[e] = 0.f;
    t = __builtin_amdgcn_mfma_f32_32x32x16_bf16(__builtin_bit_cast(bf8, y0), __builtin_bit_cast(bf8, i0), t, 0, 0, 0);
    t = __builtin_amdgcn_mfma_f32_32x32x16_bf16(__builtin_bit_cast(bf8, y1), __builtin_bit_cast(bf8, i1), t, 0, 0, 0);
#pragma unroll
    for (int a = 0; a < 2; ++a)
#pragma unroll
      for (int b = 0; b < 2; ++b)
        if (w == 2 * a + b) acc[a][b] += t;
  }
  float* red = (float*)smem;
#pragma unroll
  for (int a = 0; a < 2; ++a)
#pragma unroll
    for (int b = 0; b < 2; ++b)
#pragma unroll
      for (int g = 0; g < 4; ++g) {
        f4 v = {acc[a][b][4 * g], acc[a][b][4 * g + 1], acc[a][b][4 * g + 2], acc[a][b][4 * g + 3]};
        *(f4*)(red + (w * 64 + b * 32 + r32) * 68 + a * 32 + 8 * g + 4 * hh) = v;
      }
  WAITLGKM(); BAR();
  u4 o[2];
#pragma unroll
  for (int q = 0; q < 4; ++q) {
    const float* rp = red + tok * 68 + dc + 4 * q;
    f4 v = (*(const f4*)rp + *(const f4*)(rp + 64 * 68)) + (*(const f4*)(rp + 128 * 68) + *(const f4*)(rp + 192 * 68));
    unsigned xa = q < 2 ? xr0[2 * q] : xr1[2 * q - 4], xb = q < 2 ? xr0[2 * q + 1] : xr1[2 * q - 3];
    o[q >> 1][2 * (q & 1)] = pk_bf(v[0] + bf_lo(xa), v[1] + bf_hi(xa));
    o[q >> 1][2 * (q & 1) + 1] = pk_bf(v[2] + bf_lo(xb), v[3] + bf_hi(xb));
  }
  *(u4*)(out + oidx) = o[0];
  *(u4*)(out + oidx + 8) = o[1];
}

extern "C" void kernel_launch(void* const* d_in, const int* in_sizes, int n_in, void* d_out, int out_size, void* d_ws,
                              size_t ws_size, hipStream_t stream) {
  (void)in_sizes; (void)n_in; (void)out_size; (void)ws_size;
  typedef const unsigned short* cp;
  (void)hipFuncSetAttribute((const void*)k1_kernel, hipFuncAttributeMaxDynamicSharedMemorySize, K1_LDS);
  (void)hipFuncSetAttribute((const void*)k2_kernel, hipFuncAttributeMaxDynamicSharedMemorySize, K2_LDS);
  k1_kernel<<<256, 256, K1_LDS, stream>>>((cp)d_in[0], (cp)d_in[1], (cp)d_in[2], (cp)d_in[3], (cp)d_in[4], (cp)d_in[5],
                                          (cp)d_in[8], (cp)d_in[9], (cp)d_in[10], (char*)d_ws);
  k2_kernel<<<256, 512, K2_LDS, stream>>>((cp)d_in[0], (cp)d_in[6], (char*)d_ws, (unsigned short*)d_out);
}
```

```cpp
#include <hip/hip_runtime.h>
#include <hip/hip_bf16.h>
#include <stdint.h>

typedef __attribute__((ext_vector_type(2))) __bf16 bf2;
typedef __attribute__((ext_vector_type(8))) __bf16 bf8;
typedef __attribute__((ext_vector_type(2))) _Float16 h2;
typedef __attribute__((ext_vector_type(8))) _Float16 h8;
typedef __attribute__((ext_vector_type(2))) float f2;
typedef __attribute__((ext_vector_type(4))) float f4;
typedef __attribute__((ext_vector_type(16))) float f16v;
typedef __attribute__((ext_vector_type(2))) unsigned u2;
typedef __attribute__((ext_vector_type(4))) unsigned u4;
typedef __attribute__((ext_vector_type(4))) short s4;
#define LDSP __attribute__((address_space(3)))
#define DEVI __device__ __forceinline__

constexpr int SEQ = 2048, DM = 512, FF = 1792;
constexpr int XT_ROW = 4608;
constexpr size_t OFF_H = 0;
constexpr size_t OFF_WF = OFF_H + (size_t)64 * 112 * 1024;
constexpr size_t OFF_XT = OFF_WF + (size_t)16 * 112 * 1024;
constexpr size_t OFF_YA = OFF_XT + (size_t)512 * XT_ROW + 1024;
constexpr size_t OFF_CNT = OFF_YA + (size_t)512 * 2048 * 2;

DEVI float bf_lo(unsigned v) { return __builtin_bit_cast(float, v << 16); }
DEVI float bf_hi(unsigned v) { return __builtin_bit_cast(float, v & 0xffff0000u); }
DEVI unsigned pk_bf(float a, float b) { f2 f = {a, b}; return __builtin_bit_cast(unsigned, __builtin_convertvector(f, bf2)); }
DEVI unsigned pk_h(float a, float b) { f2 f = {a, b}; return __builtin_bit_cast(unsigned, __builtin_convertvector(f, h2)); }
DEVI float rbf(float a) { return bf_lo(pk_bf(a, 0.f)); }
DEVI float ldbf(const unsigned short* p) { return __builtin_bit_cast(float, ((unsigned)*p) << 16); }
DEVI float dot2(unsigned a, unsigned b, float c) {
  return __builtin_amdgcn_fdot2_f32_bf16(__builtin_bit_cast(bf2, a), __builtin_bit_cast(bf2, b), c, false);
}
DEVI void glds16(const void* g, unsigned lds_base) {
  unsigned sv;
  asm volatile("s_mov_b32 %0, m0\n\ts_mov_b32 m0, %2\n\ts_nop 0\n\tglobal_load_lds_dwordx4 %1, off\n\ts_mov_b32 m0, %0"
               : "=&s"(sv) : "v"(g), "s"(lds_base) : "memory");
}
DEVI void glds16s(unsigned voff, const void* sbase, unsigned lds_base) {
  asm volatile("s_mov_b32 m0, %2\n\ts_nop 0\n\tglobal_load_lds_dwordx4 %0, %1" :: "v"(voff), "s"(sbase), "s"(lds_base) : "memory", "m0");
}
#define WAITVM(n) asm volatile("s_waitcnt vmcnt(" #n ")" ::: "memory")
#define WAITLGKM() asm volatile("s_waitcnt lgkmcnt(0)" ::: "memory")
#define BAR() do { asm volatile("" ::: "memory"); __builtin_amdgcn_s_barrier(); asm volatile("" ::: "memory"); } while (0)
#define GLD(dst, voff, sptr, imm) asm volatile("global_load_dwordx4 %0, %1, %2 offset:" #imm : "=v"(dst) : "v"(voff), "s"(sptr))
#define GLDH(dst, voff, sptr) asm volatile("global_load_ushort %0, %1, %2" : "=v"(dst) : "v"(voff), "s"(sptr))
#define SB() __builtin_amdgcn_sched_barrier(0)
template <int V> struct ic { static constexpr int value = V; };
template <int I, int N, class F> DEVI void static_for(F&& f) { if constexpr (I < N) { f(ic<I>{}); static_for<I + 1, N>(f); } }
template <int N> DEVI void waitvm() { asm volatile("s_waitcnt vmcnt(%0)" ::"n"(N) : "memory"); }

#define STAMP(i)
#define STAMP_DECL
#define STAMP_OUT(k)
struct cf { float r, i; };
DEVI cf cmul(cf a, cf b) { cf c; c.r = a.r * b.r - a.i * b.i; c.i = a.r * b.i + a.i * b.r; return c; }
DEVI cf zpow(float a2, float rev, float t) {
  float e = __builtin_amdgcn_exp2f(a2 * t);
  float p = __builtin_amdgcn_fractf(rev * t);
  cf c; c.r = e * __builtin_amdgcn_cosf(p); c.i = e * __builtin_amdgcn_sinf(p); return c;
}
DEVI float wave_sum(float v) {
#pragma unroll
  for (int o = 32; o >= 1; o >>= 1) v += __shfl_xor(v, o);
  return v;
}

constexpr int K1_LDS = 10 * 16384;

DEVI void kgen_wave(char* scr, int lane, float a2, float rev, cf C) {
  char* tile = scr;
  float* tab = (float*)(scr + 4352);
  const int rd = (lane & 15) * 272 + (lane >> 4) * 16;
  cf z1 = zpow(a2, rev, 1.f), z16 = zpow(a2, rev, 16.f), z256 = zpow(a2, rev, 256.f), z1024 = zpow(a2, rev, 1024.f);
  h8 qf[4];
  {
    cf z32 = cmul(z16, z16);
    cf e = {1.f, 0.f}, o = z16;
#pragma unroll
    for (int r = 0; r < 16; r += 2) {
      *(unsigned*)(tile + r * 272 + lane * 4) = pk_h(e.r, -e.i);
      *(unsigned*)(tile + (r + 1) * 272 + lane * 4) = pk_h(o.r, -o.i);
      if (r < 14) { e = cmul(e, z32); o = cmul(o, z32); }
    }
#pragma unroll
    for (int s = 0; s < 4; ++s) qf[s] = *(const h8*)(tile + rd + s * 64);
  }
  cf P0[16];
  {
    cf z2 = cmul(z1, z1);
    P0[0] = C; P0[1] = cmul(C, z1);
#pragma unroll
    for (int e = 2; e < 16; ++e) P0[e] = cmul(P0[e - 2], z2);
  }
  const int col = lane & 15, rq = lane >> 4;
  {
#pragma unroll
    for (int r = 0; r < 16; ++r) *(unsigned*)(tile + r * 272 + lane * 4) = pk_h(P0[r].r, P0[r].i);
    f4 acc = {0.f, 0.f, 0.f, 0.f};
#pragma unroll
    for (int s = 0; s < 4; ++s) acc = __builtin_amdgcn_mfma_f32_16x16x32_f16(*(const h8*)(tile + rd + s * 64), qf[s], acc, 0, 0, 0);
    *(f4*)(tab + 4 * rq + 16 * col) = acc;
  }
  cf z512 = cmul(z256, z256), z768 = cmul(z512, z256);
  {
#pragma unroll
    for (int r = 0; r < 8; ++r) { cf p = cmul(P0[2 * r], z256); *(unsigned*)(tile + r * 272 + lane * 4) = pk_h(p.r, p.i); }
#pragma unroll
    for (int r = 8; r < 16; ++r) { cf p = cmul(P0[4 * (r & 3)], (r & 4) ? z768 : z512); *(unsigned*)(tile + r * 272 + lane * 4) = pk_h(p.r, p.i); }
    f4 acc = {0.f, 0.f, 0.f, 0.f};
#pragma unroll
    for (int s = 0; s < 4; ++s) acc = __builtin_amdgcn_mfma_f32_16x16x32_f16(*(const h8*)(tile + rd + s * 64), qf[s], acc, 0, 0, 0);
    int ci = (rq < 2) ? (256 + 4 * rq + 8 * col) : (384 + 64 * (rq & 1) + 4 * col);
    *(f4*)(tab + ci) = acc;
  }
  {
    cf w[4]; w[0] = z1024; w[1] = cmul(z1024, z256); w[2] = cmul(z1024, z512); w[3] = cmul(z1024, z768);
#pragma unroll
    for (int r = 0; r < 8; ++r) { cf p = cmul(P0[8 * (r & 1)], w[r >> 1]); *(unsigned*)(tile + r * 272 + lane * 4) = pk_h(p.r, p.i); }
    { cf p = cmul(cmul(C, z1024), z1024); *(unsigned*)(tile + 8 * 272 + lane * 4) = pk_h(p.r, p.i); }
    f4 acc = {0.f, 0.f, 0.f, 0.f};
#pragma unroll
    for (int s = 0; s < 4; ++s) acc = __builtin_amdgcn_mfma_f32_16x16x32_f16(*(const h8*)(tile + rd + s * 64), qf[s], acc, 0, 0, 0);
    if (rq < 2) {
      f2 lo = {acc[0], acc[1]}, hi = {acc[2], acc[3]};
      *(f2*)(tab + 512 + 64 * rq + 2 * col) = lo;
      *(f2*)(tab + 512 + 64 * rq + 32 + 2 * col) = hi;
    } else if (lane == 32) {
      tab[640] = acc[0];
    }
  }
}

DEVI void kput(char* kc, int blk, const float* v) {
  unsigned E[5], O[5];
#pragma unroll
  for (int k = 0; k < 5; ++k) { E[k] = pk_h(v[2 * k], v[2 * k + 1]); O[k] = pk_h(v[2 * k + 1], v[2 * k + 2]); }
  *(u4*)(kc + 16 * blk) = u4{E[0], E[1], E[2], E[3]};
  *(u4*)(kc + 4160 + 16 * blk) = u4{O[0], O[1], O[2], O[3]};
  *(u4*)(kc + 2 * 4160 + 16 * blk) = u4{E[1], E[2], E[3], E[4]};
  *(u4*)(kc + 3 * 4160 + 16 * blk) = u4{O[1], O[2], O[3], O[4]};
}
DEVI void kexpand(const float* tA, const float* tB, int t, int par, char* kc) {
  float v[11];
  {
    const int m = 255 - t;
    float z = tA[383 + m] + tB[383 + m], a = tA[384 + m] + tB[384 + m], b = tA[385 + m] + tB[385 + m];
    v[0] = b; v[1] = b; v[2] = b; v[3] = (m & 1) ? b : a; v[4] = a; v[5] = a; v[6] = a; v[7] = a; v[8] = a; v[9] = a;
    v[10] = m == 128 ? z : a;
    kput(kc, t, v);
  }
  const int m = 127 - t;
  if (par == 0) {
    const int bs = 256 + 2 * m;
    float y = tA[bs - 2] + tB[bs - 2], z = tA[bs - 1] + tB[bs - 1], a = tA[bs] + tB[bs], b = tA[bs + 1] + tB[bs + 1], c = tA[bs + 2] + tB[bs + 2];
    v[0] = c; v[1] = c; v[2] = b; v[3] = b; v[4] = b; v[5] = a; v[6] = a; v[7] = a; v[8] = a;
    v[9] = m == 64 ? z : a; v[10] = m == 64 ? y : z;
  } else {
    const bool s0 = m < 32, s1 = m <= 32, nz = m != 0;
    const int bs = s0 ? 8 * m - 3 : 125 + 4 * m;
    float W[11];
#pragma unroll
    for (int i = 0; i < 11; ++i) W[i] = tA[bs + i] + tB[bs + i];
    v[0] = s0 ? W[10] : W[7]; v[1] = s0 ? W[9] : W[6]; v[2] = s0 ? W[8] : W[5]; v[3] = s0 ? W[7] : W[5];
    v[4] = s0 ? W[6] : W[5]; v[5] = s0 ? W[5] : W[4]; v[6] = s0 ? W[4] : W[3]; v[7] = W[3];
    v[8] = nz ? (s1 ? W[2] : W[3]) : 0.f; v[9] = nz ? (s1 ? W[1] : W[2]) : 0.f; v[10] = nz ? (s1 ? W[0] : W[1]) : 0.f;
  }
  kput(kc, 128 + t, v);
}

DEVI bf8 tr_frag(unsigned addr) {
  s4 a = __builtin_amdgcn_ds_read_tr16_b64_v4i16((LDSP s4*)(uintptr_t)addr);
  s4 b = __builtin_amdgcn_ds_read_tr16_b64_v4i16((LDSP s4*)(uintptr_t)(addr + 256));
  typedef __attribute__((ext_vector_type(8))) short s8;
  s8 r = __builtin_shufflevector(a, b, 0, 1, 2, 3, 4, 5, 6, 7);
  return __builtin_bit_cast(bf8, r);
}
DEVI float ln_inv(float var) { return 1.f / rbf(__builtin_sqrtf(rbf(rbf(fmaxf(var, 0.f)) + 1e-5f))); }
constexpr int slot_of(int s) { return (s + 8) % 10; }

__global__ void __launch_bounds__(256, 1)
k1_kernel(const unsigned short* __restrict__ x, const unsigned short* __restrict__ wg, const unsigned short* __restrict__ wu,
          const unsigned short* __restrict__ wd, char* __restrict__ ws) {
  extern __shared__ __attribute__((aligned(16))) char smem[];
  const int tid = threadIdx.x, lane = tid & 63;
  const int w = __builtin_amdgcn_readfirstlane(tid >> 6);
  const int bid = blockIdx.x, xcd = bid & 7, idx = bid >> 3;
  const int mt = 4 * (xcd >> 1) + idx / 7, ft = 7 * (xcd & 1) + idx % 7;
  const unsigned lds0 = (unsigned)(uintptr_t)(LDSP char*)smem;
  const int r32 = lane & 31, hh = lane >> 5;
  STAMP_DECL; STAMP(0);

  if (idx >= 28) {
    const int sp = xcd * 4 + (idx - 28);
    if (sp < 2) { u4 z = {0u, 0u, 0u, 0u}; *(u4*)(ws + OFF_CNT + sp * 4096 + tid * 16) = z; }
    {
      const int l0 = sp * 64 + w * 16;
      u4 xv[16];
#pragma unroll
      for (int t = 0; t < 16; ++t) xv[t] = *(const u4*)(x + (size_t)(l0 + t) * DM + lane * 8);
      float sm[16], sq[16];
#pragma unroll
      for (int t = 0; t < 16; ++t)
        sm[t] = dot2(xv[t][0], 0x3f803f80u, dot2(xv[t][1], 0x3f803f80u, 0.f)) + dot2(xv[t][2], 0x3f803f80u, dot2(xv[t][3], 0x3f803f80u, 0.f));
#pragma unroll
      for (int o = 32; o >= 1; o >>= 1)
#pragma unroll
        for (int t = 0; t < 16; ++t) sm[t] += __shfl_xor(sm[t], o);
#pragma unroll
      for (int t = 0; t < 16; ++t) {
        const float mean = sm[t] * (1.f / 512.f), mb = rbf(mean);
        float q = 0.f;
#pragma unroll
        for (int p = 0; p < 4; ++p) {
          float a = bf_lo(xv[t][p]) - mb, b = bf_hi(xv[t][p]) - mb;
          q = fmaf(a, a, fmaf(b, b, q));
          xv[t][p] = pk_bf(a, b);
        }
        sq[t] = q; sm[t] = (mean - mb) * (mean - mb);
      }
#pragma unroll
      for (int o = 32; o >= 1; o >>= 1)
#pragma unroll
        for (int t = 0; t < 16; ++t) sq[t] += __shfl_xor(sq[t], o);
#pragma unroll
      for (int t = 0; t < 16; ++t) {
        const float inv = ln_inv(sq[t] * (1.f / 512.f) - sm[t]);
#pragma unroll
        for (int p = 0; p < 4; ++p) xv[t][p] = pk_bf(bf_lo(xv[t][p]) * inv, bf_hi(xv[t][p]) * inv);
      }
      char* xl = smem + lane * 1024;
      const int sa = ((2 * w) ^ (lane & 7)) * 16, sb = ((2 * w + 1) ^ (lane & 7)) * 16;
#pragma unroll
      for (int p = 0; p < 4; ++p) {
        u4 lo[2], hi[2];
#pragma unroll
        for (int t = 0; t < 8; ++t) {
          lo[t >> 2][t & 3] = pk_h(bf_lo(xv[2 * t][p]), bf_lo(xv[2 * t + 1][p]));
          hi[t >> 2][t & 3] = pk_h(bf_hi(xv[2 * t][p]), bf_hi(xv[2 * t + 1][p]));
        }
        *(u4*)(xl + (2 * p) * 128 + sa) = lo[0]; *(u4*)(xl + (2 * p) * 128 + sb) = lo[1];
        *(u4*)(xl + (2 * p + 1) * 128 + sa) = hi[0]; *(u4*)(xl + (2 * p + 1) * 128 + sb) = hi[1];
      }
      __syncthreads();
      char* xd = ws + OFF_XT + (size_t)(tid >> 3) * XT_ROW + 144 * sp + (tid & 7) * 16;
#pragma unroll
      for (int i = 0; i < 16; ++i) {
        const int c = 32 * i + (tid >> 3);
        *(u4*)(xd + (size_t)(32 * i) * XT_ROW) = *(const u4*)(smem + c * 128 + (((tid & 7) ^ ((c >> 3) & 7)) * 16));
      }
    }
    STAMP(1);
    const int fr0 = (sp * 4 + w) * 14;
#pragma unroll
    for (int b = 0; b < 2; ++b) {
      unsigned short v[7][8];
#pragma unroll
      for (int i = 0; i < 7; ++i) {
        int fr = fr0 + b * 7 + i, nt = fr / 112, ks = fr % 112;
        const unsigned short* p = wd + (size_t)(16 * ks + 4 * hh) * DM + 32 * nt + r32;
#pragma unroll
        for (int j = 0; j < 8; ++j) v[i][j] = p[(8 * (j >> 2) + (j & 3)) * DM];
      }
#pragma unroll
      for (int i = 0; i < 7; ++i) {
        u4 o;
#pragma unroll
        for (int j = 0; j < 4; ++j) o[j] = (unsigned)v[i][2 * j] | ((unsigned)v[i][2 * j + 1] << 16);
        *(u4*)(ws + OFF_WF + (size_t)(fr0 + b * 7 + i) * 1024 + lane * 16) = o;
      }
    }
    STAMP(2); STAMP_OUT(0);
    return;
  }

  const unsigned wvo = ((8 * w + ((lane >> 2) & 7)) * FF + ft * 128 + (lane >> 5) * 32 + (lane & 3) * 8) * 2;
  const unsigned xsl = lds0 + w * 4096;
  auto issue_piece = [&](int s, int q) {
    glds16s(wvo, (q & 2 ? wu : wg) + (size_t)s * 32 * FF + (q & 1) * 64, xsl + slot_of(s) * 16384 + q * 1024);
  };
  auto issue_slab = [&](int s) { issue_piece(s, 0); issue_piece(s, 1); issue_piece(s, 2); issue_piece(s, 3); };
  u4 xf[32];
  {
    unsigned xvo[4];
#pragma unroll
    for (int j = 0; j < 4; ++j) xvo[j] = (mt * 128 + w * 32 + 4 * j + (lane >> 4)) * 1024 + ((((lane & 15) ^ (lane >> 4)) ^ (4 * j)) * 16);
#pragma unroll
    for (int c = 0; c < 4; ++c) {
#pragma unroll
      for (int j = 0; j < 8; ++j) glds16s(xvo[j & 3], x + 128 * c + (j >> 2) * 8192, xsl + c * 32768 + (j >> 2) * 16384 + 1024 * (j & 3));
    }
    issue_slab(0); issue_slab(1);
  }
  STAMP(1);
  float mean, var;
  {
    const unsigned xb0 = xsl + (r32 >> 4) * 16384 + (r32 & 15) * 256;
    float s0 = 0.f, s1 = 0.f, q0 = 0.f, q1 = 0.f;
    static_for<0, 2>([&](auto H) {
      constexpr int c = 2 * H.value;
      waitvm<(c == 0 ? 24 : 16)>();
#pragma unroll
      for (int s = 0; s < 8; ++s) {
        const LDSP char* p = (const LDSP char*)(uintptr_t)(xb0 + c * 32768 + (((2 * s + hh) ^ (r32 & 15)) * 16));
        xf[8 * c + s] = *(const LDSP u4*)p; xf[8 * c + 8 + s] = *(const LDSP u4*)(p + 32768);
      }
#pragma unroll
      for (int s = 8 * c; s < 8 * c + 16; ++s)
#pragma unroll
        for (int p = 0; p < 4; p += 2) {
          s0 = dot2(xf[s][p], 0x3f803f80u, s0); s1 = dot2(xf[s][p + 1], 0x3f803f80u, s1);
          q0 = dot2(xf[s][p], xf[s][p], q0); q1 = dot2(xf[s][p + 1], xf[s][p + 1], q1);
        }
      asm volatile("" ::"v"(s0), "v"(s1), "v"(q0), "v"(q1) : "memory");
      if constexpr (c == 0) { issue_slab(2); issue_slab(3); } else { issue_slab(4); }
    });
    float t = s0 + s1, q = q0 + q1;
    t += __shfl_xor(t, 32); q += __shfl_xor(q, 32);
    mean = t * (1.f / 512.f);
    var = q * (1.f / 512.f) - mean * mean;
  }
  STAMP(2);
  const float mb = rbf(mean);
  auto center = [&](int s, int p) { xf[s][p] = pk_bf(bf_lo(xf[s][p]) - mb, bf_hi(xf[s][p]) - mb); };
#pragma unroll
  for (int p = 0; p < 8; ++p) center(p >> 2, p & 3);
  f16v acc[8];
#pragma unroll
  for (int c = 0; c < 8; ++c)
#pragma unroll
    for (int e = 0; e < 16; ++e) acc[c][e] = 0.f;
  unsigned tb[3];
  tb[0] = lds0 + hh * 4096 + ((lane & 15) >> 2) * 64 + ((lane >> 4) & 1) * 32 + (lane & 3) * 8;
  tb[1] = tb[0] + 65536; tb[2] = tb[0] + 131072;
  asm volatile("" : "+v"(tb[0]), "+v"(tb[1]), "+v"(tb[2]));
  auto wfrag = [&](int n, int c) {
    const int sl = slot_of(n >> 2), nG = n & 3;
    return tr_frag(tb[sl >> 2] + (sl & 3) * 16384 + (nG >> 1) * 8192 + (nG & 1) * 2048 + c * 512);
  };
  waitvm<16>();
  BAR();
  bf8 wf[3][4];
#pragma unroll
  for (int c = 0; c < 8; ++c) wf[c >> 2][c & 3] = wfrag(c >> 2, c & 3);
  STAMP(4);
  SB();
  static_for<0, 64>([&](auto GG) {
    constexpr int g = GG.value, i = g >> 2, G = g & 3;
    if constexpr (G == 1 && i < 15) {
      waitvm<(i <= 11 ? 12 : 4 * (14 - i))>();
      BAR();
    }
    __builtin_amdgcn_s_waitcnt(g < 63 ? 0xC87F : 0xC07F);
    SB();
    const bf8 xb = __builtin_bit_cast(bf8, xf[2 * i + (G >> 1)]);
#pragma unroll
    for (int c = 0; c < 4; ++c) {
      acc[(G & 1) * 4 + c] = __builtin_amdgcn_mfma_f32_32x32x16_bf16(wf[g % 3][c], xb, acc[(G & 1) * 4 + c], 0, 0, 0);
      if constexpr (g + 2 < 64) wf[(g + 2) % 3][c] = wfrag(g + 2, c);
      if constexpr (g >= 1 && (g - 1) / 4 + 5 <= 15) { if (c == 1) issue_piece((g - 1) / 4 + 5, (g - 1) & 3); }
      if constexpr (i < 15) { if ((c & 1) == 0) center(2 * i + 2 + (G >> 1), (G & 1) * 2 + (c >> 1)); }
      SB();
    }
  });

  STAMP(5);
  const float inv = ln_inv(var);
  const float c1 = -inv * 1.44269504088896f, c2 = inv * inv;
  char* hdst = ws + OFF_H + ((size_t)(mt * 4 + w) * 112 + ft * 8) * 1024 + lane * 16;
#pragma unroll
  for (int c = 0; c < 4; ++c) {
    unsigned o[8];
#pragma unroll
    for (int p = 0; p < 8; ++p) {
      f2 g = f2{acc[c][2 * p], acc[c][2 * p + 1]};
      f2 u = f2{acc[4 + c][2 * p], acc[4 + c][2 * p + 1]};
      f2 t = g * c1;
      f2 ex = f2{__builtin_amdgcn_exp2f(t[0]), __builtin_amdgcn_exp2f(t[1])} + 1.f;
      f2 r = f2{__builtin_amdgcn_rcpf(ex[0]), __builtin_amdgcn_rcpf(ex[1])};
      f2 hv = (g * u) * (r * c2);
      o[p] = pk_bf(hv[0], hv[1]);
    }
    u4 a = {o[0], o[1], o[2], o[3]}, b = {o[4], o[5], o[6], o[7]};
    *(u4*)(hdst + (2 * c) * 1024) = a;
    *(u4*)(hdst + (2 * c + 1) * 1024) = b;
    SB();
  }
  STAMP(6); STAMP_OUT(0);
}

constexpr int K2_RED = 4 * 64 * 272;
constexpr int C_XS = 0, C_K = 7424, C_Y = 7424 + 4 * 4160, C_SZ = C_Y + 20480;
constexpr int K2_SYNC = K2_RED + 2 * C_SZ;
constexpr int K2_LDS = K2_SYNC + 64;

DEVI void pair_sync(LDSP unsigned* c, unsigned target, int lane) {
  asm volatile("s_waitcnt vmcnt(0) lgkmcnt(0)" ::: "memory");
  if (lane == 0) __hip_atomic_fetch_add(c, 1u, __ATOMIC_RELAXED, __HIP_MEMORY_SCOPE_WORKGROUP);
  while (__hip_atomic_load(c, __ATOMIC_RELAXED, __HIP_MEMORY_SCOPE_WORKGROUP) < target) __builtin_amdgcn_s_sleep(1);
  asm volatile("" ::: "memory");
}
DEVI h8 kfrag(const LDSP char* p, const LDSP char* p8, int off) {
  u2 a = *(const LDSP u2*)(p + off), b = *(const LDSP u2*)(p8 + off);
  u4 r = {a[0], a[1], b[0], b[1]};
  return __builtin_bit_cast(h8, r);
}
constexpr int xoff(int v) { return 32 * v + 16 * (v >> 2); }

template <int U0, int U1>
DEVI void conv_near(const LDSP char* xa, const LDSP char* kb, const LDSP char* kb8, f16v& a0, f16v& a1) {
  constexpr int D = 3;
  h8 xq[D + 2], kq[D];
#pragma unroll
  for (int i = 0; i < D + 2; ++i) xq[i] = *(const LDSP h8*)(xa + xoff(U0 + i - 64));
#pragma unroll
  for (int i = 0; i < D; ++i) kq[i] = kfrag(kb, kb8, 32 * (U0 + i));
  SB();
  static_for<U0, U1>([&](auto U) {
    constexpr int u = U.value, i = u - U0;
    a0 = __builtin_amdgcn_mfma_f32_32x32x16_f16(xq[i % (D + 2)], kq[i % D], a0, 0, 0, 0);
    if constexpr (u + D < U1) xq[i % (D + 2)] = *(const LDSP h8*)(xa + xoff(u + D + 2 - 64));
    SB();
    a1 = __builtin_amdgcn_mfma_f32_32x32x16_f16(xq[(i + 2) % (D + 2)], kq[i % D], a1, 0, 0, 0);
    if constexpr (u + D < U1) kq[i % D] = kfrag(kb, kb8, 32 * (u + D));
    SB();
  });
}

__global__ void __launch_bounds__(512, 2)
k2_kernel(const unsigned short* __restrict__ x, const unsigned short* __restrict__ logA, const unsigned short* __restrict__ Aim,
          const unsigned short* __restrict__ Cre, const unsigned short* __restrict__ Cim, const unsigned short* __restrict__ logdt,
          const unsigned short* __restrict__ dskip, char* __restrict__ ws,
          unsigned short* __restrict__ out) {
  extern __shared__ __attribute__((aligned(16))) char smem[];
  const int tid = threadIdx.x, lane = tid & 63;
  const int w = __builtin_amdgcn_readfirstlane(tid >> 6);
  const int bid = blockIdx.x, xcd = bid & 7, idx = bid >> 3;
  const unsigned lds0 = (unsigned)(uintptr_t)(LDSP char*)smem;
  const int r32 = lane & 31, hh = lane >> 5;
  LDSP unsigned* sy = (LDSP unsigned*)(uintptr_t)(lds0 + K2_SYNC);
  unsigned* gcnt = (unsigned*)(ws + OFF_CNT);
  auto yrs = __builtin_amdgcn_make_buffer_rsrc((void*)(ws + OFF_YA), (short)0, 512 * 2048 * 2, 0x00020000);

  STAMP_DECL; STAMP(0);
  if (w >= 4) __builtin_amdgcn_s_setprio(3);
  if (w == 4 && lane < 16) sy[lane] = 0u;
  WAITLGKM(); BAR();
  if (w >= 4) {
    const int cl = (w - 4) >> 1, wv = w & 1, ch = 2 * bid + cl;
    char* cb = smem + K2_RED + cl * C_SZ;
    const unsigned cl0 = lds0 + K2_RED + cl * C_SZ;
    const int n0 = ch * 128 + wv * 64 + lane, n1 = n0 ^ 64;
    const float pla = ldbf(logA + n0), pai = ldbf(Aim + n0), pcr = ldbf(Cre + n0), pci = ldbf(Cim + n0);
    const float qcr = ldbf(Cre + n1), qci = ldbf(Cim + n1), pld = ldbf(logdt + ch), dsk = ldbf(dskip + ch);
    {
      const char* xsrc = ws + OFF_XT + (size_t)ch * XT_ROW + lane * 16;
      if (wv == 0) { glds16(xsrc, cl0 + 2304); glds16(xsrc + 1024, cl0 + 3328); glds16(xsrc + 2048, cl0 + 4352); }
      else { glds16(xsrc + 3072, cl0 + 5376); glds16(xsrc + 4096, cl0 + 6400); }
      u4 z = {0u, 0u, 0u, 0u};
      if (wv == 0) {
        *(u4*)(cb + lane * 16) = z; *(u4*)(cb + 1024 + lane * 16) = z;
        if (lane < 16) *(u4*)(cb + 2048 + lane * 16) = z;
      } else if (lane < 16) {
        *(u4*)(cb + C_K + (lane >> 2) * 4160 + 4096 + (lane & 3) * 16) = z;
      }
    }
    float scale;
    {
      float dt = rbf(__expf(pld));
      float are = -rbf(__expf(pla));
      float a2 = (are * dt) * 1.44269504088896f, rev = (pai * dt) * 0.159154943091895f;
      scale = 2.f * dt * wave_sum(fabsf(pcr) + fabsf(pci) + fabsf(qcr) + fabsf(qci));
      float isc = scale > 0.f ? 2.f * dt / scale : 0.f;
      cf C = {pcr * isc, pci * isc};
      STAMP(5);
      kgen_wave(cb + C_Y + wv * 8192, lane, a2, rev, C);
      STAMP(6);
      pair_sync(sy + 6 + cl, 2u, lane);
      const float* tA = (const float*)(cb + C_Y + 4352);
      kexpand(tA, tA + 2048, wv * 64 + lane, wv, cb + C_K);
    }
    pair_sync(sy + cl, 2u, lane);
    STAMP(1);
    const int cc = (3 - r32) & 3;
    const LDSP char* cbl = (const LDSP char*)(uintptr_t)cl0;
    const LDSP char* kb = cbl + C_K + cc * 4160 + 2 * (8 * hh - r32 - 1 - cc);
    const LDSP char* kb8 = kb + 8;
    asm volatile("" : "+v"(kb8));
    const LDSP char* xa = cbl + 144 * r32 + 16 * hh;
    float* yb = (float*)(cb + C_Y);
    f16v a0, a1;
#pragma unroll
    for (int e = 0; e < 16; ++e) { a0[e] = 0.f; a1[e] = 0.f; }
    if (wv == 0) {
      conv_near<82, 130>(xa, kb, kb8, a0, a1);
    } else {
      const LDSP char* xfa = cbl + 144 * (r32 >> 1) + 16 * hh + 64 * (r32 & 1);
      const LDSP char* xfb = xfa + 16 * (r32 & 1);
      constexpr int D = 4;
      h8 xq[D], kq[D];
#pragma unroll
      for (int i = 0; i < D; ++i) { xq[i] = *(const LDSP h8*)((((2 + i) & 2) ? xfb : xfa) + xoff(2 + i)); kq[i] = kfrag(kb, kb8, 32 * (2 + i)); }
      SB();
      static_for<2, 66>([&](auto U) {
        constexpr int u = U.value, i = (u - 2) % D;
        if constexpr (u & 1) a1 = __builtin_amdgcn_mfma_f32_32x32x16_f16(xq[i], kq[i], a1, 0, 0, 0);
        else a0 = __builtin_amdgcn_mfma_f32_32x32x16_f16(xq[i], kq[i], a0, 0, 0, 0);
        if constexpr (u + D < 66) { xq[i] = *(const LDSP h8*)((((u + D) & 2) ? xfb : xfa) + xoff(u + D)); kq[i] = kfrag(kb, kb8, 32 * (u + D)); }
        SB();
      });
#pragma unroll
      for (int e = 0; e < 16; ++e) { yb[4096 + 32 * ((e & 3) + 8 * (e >> 2) + 4 * hh) + r32] = a0[e] + a1[e]; a0[e] = 0.f; a1[e] = 0.f; }
      conv_near<66, 82>(xa, kb, kb8, a0, a1);
    }
#pragma unroll
    for (int e = 0; e < 16; ++e) {
      int a = (e & 3) + 8 * (e >> 2) + 4 * hh;
      yb[2048 * wv + 64 * a + r32] = a0[e];
      yb[2048 * wv + 64 * a + 32 + r32] = a1[e];
    }
    STAMP(2);
    pair_sync(sy + 2 + cl, 2u, lane);
    STAMP(3);
#pragma unroll
    for (int rnd = 0; rnd < 2; ++rnd) {
      const int l = 1024 * wv + 512 * rnd + 8 * lane;
      f4 p0 = *(const f4*)(yb + l), p1 = *(const f4*)(yb + l + 4);
      p0 += *(const f4*)(yb + 2048 + l); p1 += *(const f4*)(yb + 2048 + l + 4);
      if (wv) { p0 += *(const f4*)(yb + 3072 + l); p1 += *(const f4*)(yb + 3072 + l + 4); }
      h8 xv = *(const h8*)(cb + 2304 + 2 * l + 16 * (l >> 6));
      u4 o;
#pragma unroll
      for (int p = 0; p < 4; ++p) {
        float y0 = p < 2 ? p0[2 * p] : p1[2 * p - 4], y1 = p < 2 ? p0[2 * p + 1] : p1[2 * p - 3];
        unsigned yv = pk_bf(y0 * scale, y1 * scale);
        unsigned sv = pk_bf((float)xv[2 * p] * dsk, (float)xv[2 * p + 1] * dsk);
        o[p] = pk_bf(bf_lo(yv) + bf_lo(sv), bf_hi(yv) + bf_hi(sv));
      }
      __builtin_amdgcn_raw_buffer_store_b128(o, yrs, (ch * 2048 + l) * 2, 0, 16);
    }
    asm volatile("s_waitcnt vmcnt(0)" ::: "memory");
    unsigned old = 0u;
    if (lane == 0) old = __hip_atomic_fetch_add(sy + 4, 1u, __ATOMIC_RELAXED, __HIP_MEMORY_SCOPE_WORKGROUP);
    old = __builtin_amdgcn_readfirstlane(old);
    if (old == 3u && lane < 8) __hip_atomic_store(gcnt + ((bid >> 5) * 8 + lane) * 32 + (bid & 31), 1u, __ATOMIC_RELAXED, __HIP_MEMORY_SCOPE_AGENT);
    STAMP(4); STAMP_OUT(1);
    return;
  }

  const int mt = 4 * xcd + (idx >> 3), nt = idx & 7;
  const char* hp = ws + OFF_H + ((size_t)(2 * mt) * 112 + 28 * w) * 1024;
  const char* wp = ws + OFF_WF + ((size_t)(2 * nt) * 112 + 28 * w) * 1024;
  const unsigned vo = lane * 16;
  constexpr int PD = 7;
  u4 hb[PD][2], wb[PD][2];
  auto ldstep = [&](int s) {
    const int b = s % PD, hi = (s >> 2) * 4096;
    switch (s & 3) {
      case 0: GLD(hb[b][0], vo, hp + hi, 0); GLD(wb[b][0], vo, wp + hi, 0); GLD(hb[b][1], vo, hp + 112 * 1024 + hi, 0); GLD(wb[b][1], vo, wp + 112 * 1024 + hi, 0); break;
      case 1: GLD(hb[b][0], vo, hp + hi, 1024); GLD(wb[b][0], vo, wp + hi, 1024); GLD(hb[b][1], vo, hp + 112 * 1024 + hi, 1024); GLD(wb[b][1], vo, wp + 112 * 1024 + hi, 1024); break;
      case 2: GLD(hb[b][0], vo, hp + hi, 2048); GLD(wb[b][0], vo, wp + hi, 2048); GLD(hb[b][1], vo, hp + 112 * 1024 + hi, 2048); GLD(wb[b][1], vo, wp + 112 * 1024 + hi, 2048); break;
      default: GLD(hb[b][0], vo, hp + hi, 3072); GLD(wb[b][0], vo, wp + hi, 3072); GLD(hb[b][1], vo, hp + 112 * 1024 + hi, 3072); GLD(wb[b][1], vo, wp + 112 * 1024 + hi, 3072); break;
    }
  };
  __builtin_amdgcn_s_sleep(4);
#pragma unroll
  for (int s = 0; s < PD; ++s) ldstep(s);
  const int tok = tid >> 2, dc = (tid & 3) * 16;
  const size_t oidx = (size_t)(mt * 64 + tok) * DM + nt * 64 + dc;
  STAMP(1);
  f16v acc[2][2];
#pragma unroll
  for (int e = 0; e < 16; ++e) { acc[0][0][e] = 0.f; acc[0][1][e] = 0.f; acc[1][0][e] = 0.f; acc[1][1][e] = 0.f; }
  static_for<0, 28>([&](auto S) {
    constexpr int s = S.value, b = s % PD;
    constexpr int left = (28 - 1 - s) < (PD - 1) ? (28 - 1 - s) : (PD - 1);
    asm volatile("s_waitcnt vmcnt(%4)" : "+v"(hb[b][0]), "+v"(hb[b][1]), "+v"(wb[b][0]), "+v"(wb[b][1]) : "n"(4 * left));
    const bf8 h0 = __builtin_bit_cast(bf8, hb[b][0]), h1 = __builtin_bit_cast(bf8, hb[b][1]);
    const bf8 w0 = __builtin_bit_cast(bf8, wb[b][0]), w1 = __builtin_bit_cast(bf8, wb[b][1]);
    acc[0][0] = __builtin_amdgcn_mfma_f32_32x32x16_bf16(w0, h0, acc[0][0], 0, 0, 0);
    acc[0][1] = __builtin_amdgcn_mfma_f32_32x32x16_bf16(w0, h1, acc[0][1], 0, 0, 0);
    acc[1][0] = __builtin_amdgcn_mfma_f32_32x32x16_bf16(w1, h0, acc[1][0], 0, 0, 0);
    acc[1][1] = __builtin_amdgcn_mfma_f32_32x32x16_bf16(w1, h1, acc[1][1], 0, 0, 0);
    __builtin_amdgcn_sched_barrier(0);
    if constexpr (s + PD < 28) ldstep(s + PD);
    __builtin_amdgcn_sched_barrier(0);
  });
  u4 xr0 = *(const u4*)(x + oidx), xr1 = *(const u4*)(x + oidx + 8);
  float* red = (float*)smem;
  auto put = [&](int a, int b) {
#pragma unroll
    for (int g = 0; g < 4; ++g) {
      f4 v = {acc[a][b][4 * g], acc[a][b][4 * g + 1], acc[a][b][4 * g + 2], acc[a][b][4 * g + 3]};
      *(f4*)(red + (w * 64 + b * 32 + r32) * 68 + a * 32 + 8 * g + 4 * hh) = v;
    }
  };
  STAMP(2);
#pragma unroll
  for (int a = 0; a < 2; ++a)
#pragma unroll
    for (int b = 0; b < 2; ++b)
      if (w != 2 * a + b) put(a, b);
  {
    if (w == 0) {
      unsigned spins = 0;
      const unsigned* fl = gcnt + (nt * 8 + xcd) * 32 + r32;
      while (__builtin_amdgcn_ballot_w64(__hip_atomic_load(fl, __ATOMIC_RELAXED, __HIP_MEMORY_SCOPE_AGENT) == 0u) != 0ull) {
        __builtin_amdgcn_s_sleep(4);
        if (++spins > 20000000u) break;
      }
    }
    BAR();
    STAMP(3);
    const int dt = w >> 1, tt = w & 1;
    const int yo = ((nt * 64 + dt * 32 + r32) * 2048 + mt * 64 + tt * 32 + 8 * hh) * 2;
    u4 y0 = __builtin_amdgcn_raw_buffer_load_b128(yrs, yo, 0, 16);
    u4 y1 = __builtin_amdgcn_raw_buffer_load_b128(yrs, yo + 32, 0, 16);
    u4 i0, i1;
#pragma unroll
    for (int p = 0; p < 4; ++p) {
      int k = 8 * hh + 2 * p;
      i0[p] = (k == r32 ? 0x3f80u : 0u) | (k + 1 == r32 ? 0x3f800000u : 0u);
      i1[p] = (k + 16 == r32 ? 0x3f80u : 0u) | (k + 17 == r32 ? 0x3f800000u : 0u);
    }
    f16v t;
#pragma unroll
    for (int e = 0; e < 16; ++e) t[e] = 0.f;
    t = __builtin_amdgcn_mfma_f32_32x32x16_bf16(__builtin_bit_cast(bf8, y0), __builtin_bit_cast(bf8, i0), t, 0, 0, 0);
    t = __builtin_amdgcn_mfma_f32_32x32x16_bf16(__builtin_bit_cast(bf8, y1), __builtin_bit_cast(bf8, i1), t, 0, 0, 0);
#pragma unroll
    for (int a = 0; a < 2; ++a)
#pragma unroll
      for (int b = 0; b < 2; ++b)
        if (w == 2 * a + b) { acc[a][b] += t; put(a, b); }
  }
  STAMP(4); WAITLGKM(); BAR(); STAMP(5);
  u4 o[2];
#pragma unroll
  for (int q = 0; q < 4; ++q) {
    const float* rp = red + tok * 68 + dc + 4 * q;
    f4 v = (*(const f4*)rp + *(const f4*)(rp + 64 * 68)) + (*(const f4*)(rp + 128 * 68) + *(const f4*)(rp + 192 * 68));
    unsigned xa = q < 2 ? xr0[2 * q] : xr1[2 * q - 4], xb = q < 2 ? xr0[2 * q + 1] : xr1[2 * q - 3];
    o[q >> 1][2 * (q & 1)] = pk_bf(v[0] + bf_lo(xa), v[1] + bf_hi(xa));
    o[q >> 1][2 * (q & 1) + 1] = pk_bf(v[2] + bf_lo(xb), v[3] + bf_hi(xb));
  }
  *(u4*)(out + oidx) = o[0];
  *(u4*)(out + oidx + 8) = o[1];
  STAMP(6); STAMP_OUT(1);
}

extern "C" void kernel_launch(void* const* d_in, const int* in_sizes, int n_in, void* d_out, int out_size, void* d_ws,
                              size_t ws_size, hipStream_t stream) {
  (void)in_sizes; (void)n_in; (void)out_size; (void)ws_size;
  typedef const unsigned short* cp;
  (void)hipFuncSetAttribute((const void*)k1_kernel, hipFuncAttributeMaxDynamicSharedMemorySize, K1_LDS);
  (void)hipFuncSetAttribute((const void*)k2_kernel, hipFuncAttributeMaxDynamicSharedMemorySize, K2_LDS);
  k1_kernel<<<256, 256, K1_LDS, stream>>>((cp)d_in[0], (cp)d_in[8], (cp)d_in[9], (cp)d_in[10], (char*)d_ws);
  k2_kernel<<<256, 512, K2_LDS, stream>>>((cp)d_in[0], (cp)d_in[1], (cp)d_in[2], (cp)d_in[3], (cp)d_in[4], (cp)d_in[5],
                                          (cp)d_in[6], (char*)d_ws, (unsigned short*)d_out);
}
```

```cpp
#include <hip/hip_runtime.h>
#include <hip/hip_bf16.h>
#include <stdint.h>

typedef __attribute__((ext_vector_type(2))) __bf16 bf2;
typedef __attribute__((ext_vector_type(8))) __bf16 bf8;
typedef __attribute__((ext_vector_type(2))) _Float16 h2;
typedef __attribute__((ext_vector_type(8))) _Float16 h8;
typedef __attribute__((ext_vector_type(2))) float f2;
typedef __attribute__((ext_vector_type(4))) float f4;
typedef __attribute__((ext_vector_type(16))) float f16v;
typedef __attribute__((ext_vector_type(2))) unsigned u2;
typedef __attribute__((ext_vector_type(4))) unsigned u4;
typedef __attribute__((ext_vector_type(4))) short s4;
#define LDSP __attribute__((address_space(3)))
#define DEVI __device__ __forceinline__

constexpr int SEQ = 2048, DM = 512, FF = 1792;
constexpr int XT_ROW = 4608;
constexpr size_t OFF_H = 0;
constexpr size_t OFF_WF = OFF_H + (size_t)64 * 112 * 1024;
constexpr size_t OFF_XT = OFF_WF + (size_t)16 * 112 * 1024;
constexpr size_t OFF_YA = OFF_XT + (size_t)512 * XT_ROW + 1024;
constexpr size_t OFF_CNT = OFF_YA + (size_t)512 * 2048 * 2;

DEVI float bf_lo(unsigned v) { return __builtin_bit_cast(float, v << 16); }
DEVI float bf_hi(unsigned v) { return __builtin_bit_cast(float, v & 0xffff0000u); }
DEVI unsigned pk_bf(float a, float b) { f2 f = {a, b}; return __builtin_bit_cast(unsigned, __builtin_convertvector(f, bf2)); }
DEVI unsigned pk_h(float a, float b) { f2 f = {a, b}; return __builtin_bit_cast(unsigned, __builtin_convertvector(f, h2)); }
DEVI float rbf(float a) { return bf_lo(pk_bf(a, 0.f)); }
DEVI float ldbf(const unsigned short* p) { return __builtin_bit_cast(float, ((unsigned)*p) << 16); }
DEVI float dot2(unsigned a, unsigned b, float c) {
  return __builtin_amdgcn_fdot2_f32_bf16(__builtin_bit_cast(bf2, a), __builtin_bit_cast(bf2, b), c, false);
}
DEVI void glds16(const void* g, unsigned lds_base) {
  unsigned sv;
  asm volatile("s_mov_b32 %0, m0\n\ts_mov_b32 m0, %2\n\ts_nop 0\n\tglobal_load_lds_dwordx4 %1, off\n\ts_mov_b32 m0, %0"
               : "=&s"(sv) : "v"(g), "s"(lds_base) : "memory");
}
DEVI void glds16s(unsigned voff, const void* sbase, unsigned lds_base) {
  asm volatile("s_mov_b32 m0, %2\n\ts_nop 0\n\tglobal_load_lds_dwordx4 %0, %1" :: "v"(voff), "s"(sbase), "s"(lds_base) : "memory", "m0");
}
#define WAITVM(n) asm volatile("s_waitcnt vmcnt(" #n ")" ::: "memory")
#define WAITLGKM() asm volatile("s_waitcnt lgkmcnt(0)" ::: "memory")
#define BAR() do { asm volatile("" ::: "memory"); __builtin_amdgcn_s_barrier(); asm volatile("" ::: "memory"); } while (0)
#define GLD(dst, voff, sptr, imm) asm volatile("global_load_dwordx4 %0, %1, %2 offset:" #imm : "=v"(dst) : "v"(voff), "s"(sptr))
#define GLDH(dst, voff, sptr) asm volatile("global_load_ushort %0, %1, %2" : "=v"(dst) : "v"(voff), "s"(sptr))
#define SB() __builtin_amdgcn_sched_barrier(0)
template <int V> struct ic { static constexpr int value = V; };
template <int I, int N, class F> DEVI void static_for(F&& f) { if constexpr (I < N) { f(ic<I>{}); static_for<I + 1, N>(f); } }
template <int N> DEVI void waitvm() { asm volatile("s_waitcnt vmcnt(%0)" ::"n"(N) : "memory"); }

#define STAMP(i)
#define STAMP_DECL
#define STAMP_OUT(k)
struct cf { float r, i; };
DEVI cf cmul(cf a, cf b) { cf c; c.r = a.r * b.r - a.i * b.i; c.i = a.r * b.i + a.i * b.r; return c; }
DEVI cf zpow(float a2, float rev, float t) {
  float e = __builtin_amdgcn_exp2f(a2 * t);
  float p = __builtin_amdgcn_fractf(rev * t);
  cf c; c.r = e * __builtin_amdgcn_cosf(p); c.i = e * __builtin_amdgcn_sinf(p); return c;
}
DEVI float wave_sum(float v) {
#pragma unroll
  for (int o = 32; o >= 1; o >>= 1) v += __shfl_xor(v, o);
  return v;
}

constexpr int K1_LDS = 10 * 16384;

DEVI void kgen_wave(char* sa, char* sb, int lane, float a2, float rev, cf C) {
  char* wq = sa + lane * 4; char* w0 = wq + 4352; char* w1 = sb + lane * 4; char* w2 = w1 + 4352;
  float* tab = (float*)(sa + 64);
  const int rd = (lane & 15) * 272 + (lane >> 4) * 16;
  cf z1 = zpow(a2, rev, 1.f), z16 = zpow(a2, rev, 16.f), z256 = zpow(a2, rev, 256.f), z1024 = zpow(a2, rev, 1024.f);
  const cf z32 = cmul(z16, z16), z2 = cmul(z1, z1), z512 = cmul(z256, z256), z768 = cmul(z512, z256);
  cf wz[4]; wz[0] = z1024; wz[1] = cmul(z1024, z256); wz[2] = cmul(z1024, z512); wz[3] = cmul(z1024, z768);
  cf e = {1.f, 0.f}, o = z16, pe = C, po = cmul(C, z1);
  { cf p = cmul(cmul(C, z1024), z1024); *(unsigned*)(w2 + 8 * 272) = pk_h(p.r, p.i); }
#pragma unroll
  for (int r = 0; r < 8; ++r) {
    *(unsigned*)(wq + (2 * r) * 272) = pk_h(e.r, -e.i);
    *(unsigned*)(wq + (2 * r + 1) * 272) = pk_h(o.r, -o.i);
    *(unsigned*)(w0 + (2 * r) * 272) = pk_h(pe.r, pe.i);
    *(unsigned*)(w0 + (2 * r + 1) * 272) = pk_h(po.r, po.i);
    { cf p = cmul(pe, z256); *(unsigned*)(w1 + r * 272) = pk_h(p.r, p.i); }
    if ((r & 1) == 0) {
      cf p = cmul(pe, z512), q = cmul(pe, z768);
      *(unsigned*)(w1 + (8 + (r >> 1)) * 272) = pk_h(p.r, p.i); *(unsigned*)(w1 + (12 + (r >> 1)) * 272) = pk_h(q.r, q.i);
    }
    if ((r & 3) == 0) {
#pragma unroll
      for (int f = 0; f < 4; ++f) { cf p = cmul(pe, wz[f]); *(unsigned*)(w2 + (2 * f + (r >> 2)) * 272) = pk_h(p.r, p.i); }
    }
    if (r < 7) { e = cmul(e, z32); o = cmul(o, z32); pe = cmul(pe, z2); po = cmul(po, z2); }
  }
  f4 a0 = {0.f, 0.f, 0.f, 0.f}, a1 = a0, a2v = a0;
#pragma unroll
  for (int s = 0; s < 4; ++s) {
    const h8 qf = *(const h8*)(sa + rd + s * 64);
    a0 = __builtin_amdgcn_mfma_f32_16x16x32_f16(*(const h8*)(sa + 4352 + rd + s * 64), qf, a0, 0, 0, 0);
    a1 = __builtin_amdgcn_mfma_f32_16x16x32_f16(*(const h8*)(sb + rd + s * 64), qf, a1, 0, 0, 0);
    a2v = __builtin_amdgcn_mfma_f32_16x16x32_f16(*(const h8*)(sb + 4352 + rd + s * 64), qf, a2v, 0, 0, 0);
  }
  const int col = lane & 15, rq = lane >> 4;
  *(f4*)(tab + 4 * rq + 16 * col) = a0;
  *(f4*)(tab + ((rq < 2) ? (256 + 4 * rq + 8 * col) : (384 + 64 * (rq & 1) + 4 * col))) = a1;
  if (rq < 2) {
    *(f2*)(tab + 512 + 64 * rq + 2 * col) = f2{a2v[0], a2v[1]};
    *(f2*)(tab + 512 + 64 * rq + 32 + 2 * col) = f2{a2v[2], a2v[3]};
  } else if (lane == 32) {
    tab[640] = a2v[0];
  }
}

DEVI void kput(char* kc, int blk, const float* v) {
  unsigned E[5], O[5];
#pragma unroll
  for (int k = 0; k < 5; ++k) { E[k] = pk_h(v[2 * k], v[2 * k + 1]); O[k] = pk_h(v[2 * k + 1], v[2 * k + 2]); }
  *(u4*)(kc + 16 * blk) = u4{E[0], E[1], E[2], E[3]};
  *(u4*)(kc + 4160 + 16 * blk) = u4{O[0], O[1], O[2], O[3]};
  *(u4*)(kc + 2 * 4160 + 16 * blk) = u4{E[1], E[2], E[3], E[4]};
  *(u4*)(kc + 3 * 4160 + 16 * blk) = u4{O[1], O[2], O[3], O[4]};
}
DEVI void kexpand(const float* tA, const float* tB, int t, int par, char* kc) {
  float v[11];
  {
    const int m = 255 - t;
    float z = tA[383 + m] + tB[383 + m], a = tA[384 + m] + tB[384 + m], b = tA[385 + m] + tB[385 + m];
    v[0] = b; v[1] = b; v[2] = b; v[3] = (m & 1) ? b : a; v[4] = a; v[5] = a; v[6] = a; v[7] = a; v[8] = a; v[9] = a;
    v[10] = m == 128 ? z : a;
    kput(kc, t, v);
  }
  const int m = 127 - t;
  if (par == 0) {
    const int bs = 256 + 2 * m;
    float y = tA[bs - 2] + tB[bs - 2], z = tA[bs - 1] + tB[bs - 1], a = tA[bs] + tB[bs], b = tA[bs + 1] + tB[bs + 1], c = tA[bs + 2] + tB[bs + 2];
    v[0] = c; v[1] = c; v[2] = b; v[3] = b; v[4] = b; v[5] = a; v[6] = a; v[7] = a; v[8] = a;
    v[9] = m == 64 ? z : a; v[10] = m == 64 ? y : z;
  } else {
    const bool s0 = m < 32, s1 = m <= 32, nz = m != 0;
    const int bs = s0 ? 8 * m - 3 : 125 + 4 * m;
    float W[11];
#pragma unroll
    for (int i = 0; i < 11; ++i) W[i] = tA[bs + i] + tB[bs + i];
    v[0] = s0 ? W[10] : W[7]; v[1] = s0 ? W[9] : W[6]; v[2] = s0 ? W[8] : W[5]; v[3] = s0 ? W[7] : W[5];
    v[4] = s0 ? W[6] : W[5]; v[5] = s0 ? W[5] : W[4]; v[6] = s0 ? W[4] : W[3]; v[7] = W[3];
    v[8] = nz ? (s1 ? W[2] : W[3]) : 0.f; v[9] = nz ? (s1 ? W[1] : W[2]) : 0.f; v[10] = nz ? (s1 ? W[0] : W[1]) : 0.f;
  }
  kput(kc, 128 + t, v);
}

DEVI bf8 tr_frag(unsigned addr) {
  s4 a = __builtin_amdgcn_ds_read_tr16_b64_v4i16((LDSP s4*)(uintptr_t)addr);
  s4 b = __builtin_amdgcn_ds_read_tr16_b64_v4i16((LDSP s4*)(uintptr_t)(addr + 256));
  typedef __attribute__((ext_vector_type(8))) short s8;
  s8 r = __builtin_shufflevector(a, b, 0, 1, 2, 3, 4, 5, 6, 7);
  return __builtin_bit_cast(bf8, r);
}
DEVI float ln_inv(float var) { return 1.f / rbf(__builtin_sqrtf(rbf(rbf(fmaxf(var, 0.f)) + 1e-5f))); }

constexpr int slot_of(int s) { return (s + 8) % 10; }

__global__ void __launch_bounds__(256, 1)
k1_kernel(const unsigned short* __restrict__ x, const unsigned short* __restrict__ wg, const unsigned short* __restrict__ wu,
          const unsigned short* __restrict__ wd, char* __restrict__ ws) {
  extern __shared__ __attribute__((aligned(16))) char smem[];
  const int tid = threadIdx.x, lane = tid & 63;
  const int w = __builtin_amdgcn_readfirstlane(tid >> 6);
  const int bid = blockIdx.x, xcd = bid & 7, idx = bid >> 3;
  const int mt = 4 * (xcd >> 1) + idx / 7, ft = 7 * (xcd & 1) + idx % 7;
  const unsigned lds0 = (unsigned)(uintptr_t)(LDSP char*)smem;
  const int r32 = lane & 31, hh = lane >> 5;
  STAMP_DECL; STAMP(0);
  {
    const u4 m = {~0u, ~0u, ~0u, ~0u};
    *(u4*)(ws + OFF_YA + bid * 8192 + tid * 16) = m; *(u4*)(ws + OFF_YA + bid * 8192 + 4096 + tid * 16) = m;
  }

  if (idx >= 28) {
    const int sp = xcd * 4 + (idx - 28);
    {
      const unsigned short* xp = x + (size_t)(sp * 64 + lane) * DM + w * 128;
      u4 xv[16];
#pragma unroll
      for (int j = 0; j < 16; ++j) xv[j] = *(const u4*)(xp + 8 * j);
      float s0 = 0.f, s1 = 0.f, q0 = 0.f, q1 = 0.f;
#pragma unroll
      for (int j = 0; j < 16; ++j)
#pragma unroll
        for (int p = 0; p < 4; p += 2) {
          s0 = dot2(xv[j][p], 0x3f803f80u, s0); s1 = dot2(xv[j][p + 1], 0x3f803f80u, s1);
          q0 = dot2(xv[j][p], xv[j][p], q0); q1 = dot2(xv[j][p + 1], xv[j][p + 1], q1);
        }
      f2* st2 = (f2*)smem;
      st2[w * 64 + lane] = f2{s0 + s1, q0 + q1};
      STAMP(3);
      __syncthreads();
      const f2 tt = (st2[lane] + st2[64 + lane]) + (st2[128 + lane] + st2[192 + lane]);
      const float mean = tt[0] * (1.f / 512.f), mb = rbf(mean);
      const float inv = ln_inv(tt[1] * (1.f / 512.f) - mean * mean);
      char* xd = ws + OFF_XT + (size_t)(w * 128) * XT_ROW + 144 * sp;
      const unsigned xo = (lane & 1) * XT_ROW + (lane >> 1) * 4, sel = (lane & 1) ? 0x03020706u : 0x05040100u;
#pragma unroll
      for (int j = 0; j < 16; ++j)
#pragma unroll
        for (int p = 0; p < 4; ++p) {
          unsigned d = pk_bf(bf_lo(xv[j][p]) - mb, bf_hi(xv[j][p]) - mb);
          unsigned n = pk_bf(bf_lo(d) * inv, bf_hi(d) * inv);
          unsigned hv = pk_h(bf_lo(n), bf_hi(n));
          unsigned ot = __builtin_amdgcn_update_dpp(0u, hv, 0xB1, 0xf, 0xf, false);
          *(unsigned*)(xd + (size_t)(8 * j + 2 * p) * XT_ROW + xo) = __builtin_amdgcn_perm(ot, hv, sel);
        }
    }
    STAMP(1);
    const int fr0 = (sp * 4 + w) * 14;
#pragma unroll 1
    for (int b = 0; b < 2; ++b) {
      unsigned short v[7][8];
      const int fr = fr0 + b * 7, nt = fr / 112, ks = fr % 112;
      const unsigned short* p = wd + (size_t)(16 * ks + 4 * hh) * DM + 32 * nt + r32;
#pragma unroll
      for (int i = 0; i < 7; ++i)
#pragma unroll
        for (int j = 0; j < 8; ++j) v[i][j] = p[(16 * i + 8 * (j >> 2) + (j & 3)) * DM];
#pragma unroll
      for (int i = 0; i < 7; ++i) {
        u4 o;
#pragma unroll
        for (int j = 0; j < 4; ++j) o[j] = (unsigned)v[i][2 * j] | ((unsigned)v[i][2 * j + 1] << 16);
        *(u4*)(ws + OFF_WF + (size_t)(fr + i) * 1024 + lane * 16) = o;
      }
    }
    STAMP(2); STAMP_OUT(0);
    return;
  }

  const unsigned wvo = ((8 * w + ((lane >> 2) & 7)) * FF + ft * 128 + (lane >> 5) * 32 + (lane & 3) * 8) * 2;
  const unsigned xsl = lds0 + w * 4096;
  auto issue_piece = [&](int s, int q) {
    glds16s(wvo, (q & 2 ? wu : wg) + (size_t)s * 32 * FF + (q & 1) * 64, xsl + slot_of(s) * 16384 + q * 1024);
  };
  auto issue_slab = [&](int s) { issue_piece(s, 0); issue_piece(s, 1); issue_piece(s, 2); issue_piece(s, 3); };
  u4 xf[32];
  float mean, var;
  {
    unsigned xvo[4];
#pragma unroll
    for (int j = 0; j < 4; ++j) xvo[j] = (mt * 128 + w * 32 + 4 * j + (lane >> 4)) * 1024 + ((((lane & 15) ^ (lane >> 4)) ^ (4 * j)) * 16);
    auto issue_x = [&](int c) {
#pragma unroll
      for (int j = 0; j < 8; ++j) glds16s(xvo[j & 3], x + 128 * c + (j >> 2) * 8192, xsl + c * 32768 + (j >> 2) * 16384 + 1024 * (j & 3));
    };
    issue_x(0); issue_x(1); issue_x(2);
    unsigned xb0 = xsl + (r32 >> 4) * 16384 + (r32 & 15) * 256, xb1 = xb0 + 65536;
    asm volatile("" : "+v"(xb0), "+v"(xb1));
    float s0 = 0.f, s1 = 0.f, q0 = 0.f, q1 = 0.f;
    static_for<0, 4>([&](auto H) {
      constexpr int c = H.value;
      waitvm<16>();
#pragma unroll
      for (int s = 0; s < 8; ++s)
        xf[8 * c + s] = *(const LDSP u4*)(uintptr_t)(((c & 2) ? xb1 : xb0) + (((2 * s + hh) ^ (r32 & 15)) * 16) + (c & 1) * 32768);
      if constexpr (c == 0) issue_x(3);
      if constexpr (c == 1) { issue_slab(0); issue_slab(1); }
#pragma unroll
      for (int s = 8 * c; s < 8 * c + 8; ++s)
#pragma unroll
        for (int p = 0; p < 4; p += 2) {
          s0 = dot2(xf[s][p], 0x3f803f80u, s0); s1 = dot2(xf[s][p + 1], 0x3f803f80u, s1);
          q0 = dot2(xf[s][p], xf[s][p], q0); q1 = dot2(xf[s][p + 1], xf[s][p + 1], q1);
        }
      asm volatile("" ::"v"(s0), "v"(s1), "v"(q0), "v"(q1) : "memory");
      if constexpr (c == 2) { issue_slab(2); issue_slab(3); }
      if constexpr (c == 3) issue_slab(4);
    });
    STAMP(1);
    float t = s0 + s1, q = q0 + q1;
    t += __shfl_xor(t, 32); q += __shfl_xor(q, 32);
    mean = t * (1.f / 512.f);
    var = q * (1.f / 512.f) - mean * mean;
  }
  STAMP(2);
  const float mb = rbf(mean);
  auto center = [&](int s, int p) { xf[s][p] = pk_bf(bf_lo(xf[s][p]) - mb, bf_hi(xf[s][p]) - mb); };
#pragma unroll
  for (int p = 0; p < 8; ++p) center(p >> 2, p & 3);
  f16v acc[8];
#pragma unroll
  for (int c = 0; c < 8; ++c)
#pragma unroll
    for (int e = 0; e < 16; ++e) acc[c][e] = 0.f;
  unsigned tb[3];
  tb[0] = lds0 + hh * 4096 + ((lane & 15) >> 2) * 64 + ((lane >> 4) & 1) * 32 + (lane & 3) * 8;
  tb[1] = tb[0] + 65536; tb[2] = tb[0] + 131072;
  asm volatile("" : "+v"(tb[0]), "+v"(tb[1]), "+v"(tb[2]));
  auto wfrag = [&](int n, int c) {
    const int sl = slot_of(n >> 2), nG = n & 3;
    return tr_frag(tb[sl >> 2] + (sl & 3) * 16384 + (nG >> 1) * 8192 + (nG & 1) * 2048 + c * 512);
  };
  waitvm<16>();
  BAR();
  bf8 wf[3][4];
#pragma unroll
  for (int c = 0; c < 8; ++c) wf[c >> 2][c & 3] = wfrag(c >> 2, c & 3);
  STAMP(4);
  SB();
  static_for<0, 64>([&](auto GG) {
    constexpr int g = GG.value, i = g >> 2, G = g & 3;
    if constexpr (G == 1 && i < 15) {
      waitvm<(i <= 11 ? 12 : 4 * (14 - i))>();
      BAR();
    }
    __builtin_amdgcn_s_waitcnt(g < 63 ? 0xC87F : 0xC07F);
    SB();
    const bf8 xb = __builtin_bit_cast(bf8, xf[2 * i + (G >> 1)]);
#pragma unroll
    for (int c = 0; c < 4; ++c) {
      acc[(G & 1) * 4 + c] = __builtin_amdgcn_mfma_f32_32x32x16_bf16(wf[g % 3][c], xb, acc[(G & 1) * 4 + c], 0, 0, 0);
      if constexpr (g + 2 < 64) wf[(g + 2) % 3][c] = wfrag(g + 2, c);
      if constexpr (g >= 1 && (g - 1) / 4 + 5 <= 15) { if (c == 1) issue_piece((g - 1) / 4 + 5, (g - 1) & 3); }
      if constexpr (i < 15) { if ((c & 1) == 0) center(2 * i + 2 + (G >> 1), (G & 1) * 2 + (c >> 1)); }
      SB();
    }
  });

  STAMP(5);
  const float inv = ln_inv(var);
  const float c1 = -inv * 1.44269504088896f, c2 = inv * inv;
  char* hdst = ws + OFF_H + ((size_t)(mt * 4 + w) * 112 + ft * 8) * 1024 + lane * 16;
#pragma unroll
  for (int c = 0; c < 4; ++c) {
    unsigned o[8];
#pragma unroll
    for (int p = 0; p < 8; ++p) {
      f2 g = f2{acc[c][2 * p], acc[c][2 * p + 1]};
      f2 u = f2{acc[4 + c][2 * p], acc[4 + c][2 * p + 1]};
      f2 t = g * c1;
      f2 ex = f2{__builtin_amdgcn_exp2f(t[0]), __builtin_amdgcn_exp2f(t[1])} + 1.f;
      f2 r = f2{__builtin_amdgcn_rcpf(ex[0]), __builtin_amdgcn_rcpf(ex[1])};
      f2 hv = (g * u) * (r * c2);
      o[p] = pk_bf(hv[0], hv[1]);
    }
    u4 a = {o[0], o[1], o[2], o[3]}, b = {o[4], o[5], o[6], o[7]};
    *(u4*)(hdst + (2 * c) * 1024) = a;
    *(u4*)(hdst + (2 * c + 1) * 1024) = b;
    SB();
  }
  STAMP(6); STAMP_OUT(0);
}

constexpr int K2_RED = 4 * 64 * 272;
constexpr int C_XS = 0, C_K = 7424, C_Y = 7424 + 4 * 4160, C_SZ = C_Y + 20480;
constexpr int K2_SYNC = K2_RED + 2 * C_SZ;
constexpr int K2_LDS = K2_SYNC + 64;

DEVI void pair_sync(LDSP unsigned* c, unsigned target, int lane) {
  asm volatile("s_waitcnt vmcnt(0) lgkmcnt(0)" ::: "memory");
  if (lane == 0) __hip_atomic_fetch_add(c, 1u, __ATOMIC_RELAXED, __HIP_MEMORY_SCOPE_WORKGROUP);
  while (__hip_atomic_load(c, __ATOMIC_RELAXED, __HIP_MEMORY_SCOPE_WORKGROUP) < target) __builtin_amdgcn_s_sleep(1);
  asm volatile("" ::: "memory");
}
DEVI h8 kfrag(const LDSP char* p, const LDSP char* p8, int off) {
  u2 a = *(const LDSP u2*)(p + off), b = *(const LDSP u2*)(p8 + off);
  u4 r = {a[0], a[1], b[0], b[1]};
  return __builtin_bit_cast(h8, r);
}
constexpr int xoff(int v) { return 32 * v + 16 * (v >> 2); }

template <int U0, int U1>
DEVI void conv_near(const LDSP char* xa, const LDSP char* kb, const LDSP char* kb8, f16v& a0, f16v& a1) {
  constexpr int D = 3;
  h8 xq[D + 2], kq[D];
#pragma unroll
  for (int i = 0; i < D + 2; ++i) xq[i] = *(const LDSP h8*)(xa + xoff(U0 + i - 64));
#pragma unroll
  for (int i = 0; i < D; ++i) kq[i] = kfrag(kb, kb8, 32 * (U0 + i));
  SB();
  static_for<U0, U1>([&](auto U) {
    constexpr int u = U.value, i = u - U0;
    a0 = __builtin_amdgcn_mfma_f32_32x32x16_f16(xq[i % (D + 2)], kq[i % D], a0, 0, 0, 0);
    if constexpr (u + D < U1) xq[i % (D + 2)] = *(const LDSP h8*)(xa + xoff(u + D + 2 - 64));
    SB();
    a1 = __builtin_amdgcn_mfma_f32_32x32x16_f16(xq[(i + 2) % (D + 2)], kq[i % D], a1, 0, 0, 0);
    if constexpr (u + D < U1) kq[i % D] = kfrag(kb, kb8, 32 * (u + D));
    SB();
  });
}

__global__ void __launch_bounds__(512, 2)
k2_kernel(const unsigned short* __restrict__ x, const unsigned short* __restrict__ logA, const unsigned short* __restrict__ Aim,
          const unsigned short* __restrict__ Cre, const unsigned short* __restrict__ Cim, const unsigned short* __restrict__ logdt,
          const unsigned short* __restrict__ dskip, char* __restrict__ ws,
          unsigned short* __restrict__ out) {
  extern __shared__ __attribute__((aligned(16))) char smem[];
  const int tid = threadIdx.x, lane = tid & 63;
  const int w = __builtin_amdgcn_readfirstlane(tid >> 6);
  const int bid = blockIdx.x, xcd = bid & 7, idx = bid >> 3;
  const unsigned lds0 = (unsigned)(uintptr_t)(LDSP char*)smem;
  const int r32 = lane & 31, hh = lane >> 5;
  LDSP unsigned* sy = (LDSP unsigned*)(uintptr_t)(lds0 + K2_SYNC);
  auto yrs = __builtin_amdgcn_make_buffer_rsrc((void*)(ws + OFF_YA), (short)0, 512 * 2048 * 2, 0x00020000);

  STAMP_DECL; STAMP(0);
  unsigned pla_ = 0, pai_ = 0, pcr_ = 0, pci_ = 0, qcr_ = 0, qci_ = 0, pld_ = 0, dsk_ = 0;
  if (w >= 4) {
    __builtin_amdgcn_s_setprio(3);
    const unsigned nc = (2 * bid + ((w - 4) >> 1)) * 2, n0 = nc * 128 + ((w & 1) * 64 + lane) * 2, n1 = n0 ^ 128u;
    GLDH(pla_, n0, logA); GLDH(pai_, n0, Aim); GLDH(pcr_, n0, Cre); GLDH(pci_, n0, Cim);
    GLDH(qcr_, n1, Cre); GLDH(qci_, n1, Cim); GLDH(pld_, nc, logdt); GLDH(dsk_, nc, dskip);
  }
  if (w == 4 && lane < 16) sy[lane] = 0u;
  WAITLGKM(); BAR();
  if (w >= 4) {
    const int cl = (w - 4) >> 1, wv = w & 1, ch = 2 * bid + cl;
    char* cb = smem + K2_RED + cl * C_SZ;
    const unsigned cl0 = lds0 + K2_RED + cl * C_SZ;
    {
      const char* xsrc = ws + OFF_XT + (size_t)ch * XT_ROW + lane * 16;
      if (wv == 0) { glds16(xsrc, cl0 + 2304); glds16(xsrc + 1024, cl0 + 3328); glds16(xsrc + 2048, cl0 + 4352); }
      else { glds16(xsrc + 3072, cl0 + 5376); glds16(xsrc + 4096, cl0 + 6400); glds16(xsrc + 2048, cl0 + 4352); }
      u4 z = {0u, 0u, 0u, 0u};
      if (wv == 0) {
        *(u4*)(cb + lane * 16) = z; *(u4*)(cb + 1024 + lane * 16) = z;
        if (lane < 16) *(u4*)(cb + 2048 + lane * 16) = z;
      }
    }
    asm volatile("s_waitcnt vmcnt(3)" : "+v"(pla_), "+v"(pai_), "+v"(pcr_), "+v"(pci_), "+v"(qcr_), "+v"(qci_), "+v"(pld_), "+v"(dsk_));
    const float pla = bf_lo(pla_), pai = bf_lo(pai_), pcr = bf_lo(pcr_), pci = bf_lo(pci_), qcr = bf_lo(qcr_), qci = bf_lo(qci_);
    const float pld = bf_lo(pld_), dsk = bf_lo(dsk_);
    float scale;
    {
      float dt = rbf(__expf(pld));
      float are = -rbf(__expf(pla));
      float a2 = (are * dt) * 1.44269504088896f, rev = (pai * dt) * 0.159154943091895f;
      scale = 2.f * dt * wave_sum(fabsf(pcr) + fabsf(pci) + fabsf(qcr) + fabsf(qci));
      float isc = scale > 0.f ? 2.f * dt / scale : 0.f;
      cf C = {pcr * isc, pci * isc};
      STAMP(5);
      kgen_wave(cb + C_Y + wv * 10240, cb + C_K + wv * 8320, lane, a2, rev, C);
      STAMP(6);
      pair_sync(sy + 6 + cl, 2u, lane);
      const float* tA = (const float*)(cb + C_Y + 64);
      kexpand(tA, tA + 2560, wv * 64 + lane, wv, cb + C_K);
      if (wv == 0 && lane < 16) *(u4*)(cb + C_K + (lane >> 2) * 4160 + 4096 + (lane & 3) * 16) = u4{0u, 0u, 0u, 0u};
    }
    pair_sync(sy + cl, 2u, lane);
    STAMP(1);
    const int cc = (3 - r32) & 3;
    const LDSP char* cbl = (const LDSP char*)(uintptr_t)cl0;
    const LDSP char* kb = cbl + C_K + cc * 4160 + 2 * (8 * hh - r32 - 1 - cc);
    const LDSP char* kb8 = kb + 8;
    asm volatile("" : "+v"(kb8));
    const LDSP char* xa = cbl + 144 * r32 + 16 * hh;
    float* yb = (float*)(cb + C_Y);
    f16v a0, a1;
#pragma unroll
    for (int e = 0; e < 16; ++e) { a0[e] = 0.f; a1[e] = 0.f; }
    if (wv == 0) {
      conv_near<82, 130>(xa, kb, kb8, a0, a1);
    } else {
      const LDSP char* xfa = cbl + 144 * (r32 >> 1) + 16 * hh + 64 * (r32 & 1);
      const LDSP char* xfb = xfa + 16 * (r32 & 1);
      constexpr int D = 4;
      h8 xq[D], kq[D];
#pragma unroll
      for (int i = 0; i < D; ++i) { xq[i] = *(const LDSP h8*)((((2 + i) & 2) ? xfb : xfa) + xoff(2 + i)); kq[i] = kfrag(kb, kb8, 32 * (2 + i)); }
      SB();
      static_for<2, 66>([&](auto U) {
        constexpr int u = U.value, i = (u - 2) % D;
        if constexpr (u & 1) a1 = __builtin_amdgcn_mfma_f32_32x32x16_f16(xq[i], kq[i], a1, 0, 0, 0);
        else a0 = __builtin_amdgcn_mfma_f32_32x32x16_f16(xq[i], kq[i], a0, 0, 0, 0);
        if constexpr (u + D < 66) { xq[i] = *(const LDSP h8*)((((u + D) & 2) ? xfb : xfa) + xoff(u + D)); kq[i] = kfrag(kb, kb8, 32 * (u + D)); }
        SB();
      });
#pragma unroll
      for (int e = 0; e < 16; ++e) { yb[4096 + 32 * ((e & 3) + 8 * (e >> 2) + 4 * hh) + r32] = a0[e] + a1[e]; a0[e] = 0.f; a1[e] = 0.f; }
      conv_near<66, 82>(xa, kb, kb8, a0, a1);
    }
#pragma unroll
    for (int e = 0; e < 16; ++e) {
      int a = (e & 3) + 8 * (e >> 2) + 4 * hh;
      yb[2048 * wv + 64 * a + r32] = a0[e];
      yb[2048 * wv + 64 * a + 32 + r32] = a1[e];
    }
    STAMP(2);
    pair_sync(sy + 2 + cl, 2u, lane);
    STAMP(3);
#pragma unroll
    for (int rnd = 0; rnd < 2; ++rnd) {
      const int l = 1024 * wv + 512 * rnd + 8 * lane;
      f4 p0 = *(const f4*)(yb + l), p1 = *(const f4*)(yb + l + 4);
      p0 += *(const f4*)(yb + 2048 + l); p1 += *(const f4*)(yb + 2048 + l + 4);
      if (wv) { p0 += *(const f4*)(yb + 3072 + l); p1 += *(const f4*)(yb + 3072 + l + 4); }
      h8 xv = *(const h8*)(cb + 2304 + 2 * l + 16 * (l >> 6));
      u4 o;
#pragma unroll
      for (int p = 0; p < 4; ++p) {
        float y0 = p < 2 ? p0[2 * p] : p1[2 * p - 4], y1 = p < 2 ? p0[2 * p + 1] : p1[2 * p - 3];
        unsigned yv = pk_bf(y0 * scale, y1 * scale);
        unsigned sv = pk_bf((float)xv[2 * p] * dsk, (float)xv[2 * p + 1] * dsk);
        o[p] = pk_bf(bf_lo(yv) + bf_lo(sv), bf_hi(yv) + bf_hi(sv));
        o[p] = o[p] == 0xffffffffu ? 0xfffeffffu : o[p];
      }
      __builtin_amdgcn_raw_buffer_store_b128(o, yrs, (ch * 2048 + l) * 2, 0, 16);
    }
    STAMP(4); STAMP_OUT(1);
    return;
  }

  const int mt = 4 * xcd + (idx >> 3), nt = idx & 7;
  const char* hp = ws + OFF_H + ((size_t)(2 * mt) * 112 + 28 * w) * 1024;
  const char* wp = ws + OFF_WF + ((size_t)(2 * nt) * 112 + 28 * w) * 1024;
  const unsigned vo = lane * 16;
  constexpr int PD = 7;
  u4 hb[PD][2], wb[PD][2];
  auto ldstep = [&](int s) {
    const int b = s % PD, hi = (s >> 2) * 4096;
    switch (s & 3) {
      case 0: GLD(hb[b][0], vo, hp + hi, 0); GLD(wb[b][0], vo, wp + hi, 0); GLD(hb[b][1], vo, hp + 112 * 1024 + hi, 0); GLD(wb[b][1], vo, wp + 112 * 1024 + hi, 0); break;
      case 1: GLD(hb[b][0], vo, hp + hi, 1024); GLD(wb[b][0], vo, wp + hi, 1024); GLD(hb[b][1], vo, hp + 112 * 1024 + hi, 1024); GLD(wb[b][1], vo, wp + 112 * 1024 + hi, 1024); break;
      case 2: GLD(hb[b][0], vo, hp + hi, 2048); GLD(wb[b][0], vo, wp + hi, 2048); GLD(hb[b][1], vo, hp + 112 * 1024 + hi, 2048); GLD(wb[b][1], vo, wp + 112 * 1024 + hi, 2048); break;
      default: GLD(hb[b][0], vo, hp + hi, 3072); GLD(wb[b][0], vo, wp + hi, 3072); GLD(hb[b][1], vo, hp + 112 * 1024 + hi, 3072); GLD(wb[b][1], vo, wp + 112 * 1024 + hi, 3072); break;
    }
  };
  __builtin_amdgcn_s_sleep(8);
#pragma unroll
  for (int s = 0; s < PD; ++s) ldstep(s);
  const int tok = tid >> 2, dc = (tid & 3) * 16;
  const size_t oidx = (size_t)(mt * 64 + tok) * DM + nt * 64 + dc;
  STAMP(1);
  f16v acc[2][2];
#pragma unroll
  for (int e = 0; e < 16; ++e) { acc[0][0][e] = 0.f; acc[0][1][e] = 0.f; acc[1][0][e] = 0.f; acc[1][1][e] = 0.f; }
  static_for<0, 28>([&](auto S) {
    constexpr int s = S.value, b = s % PD;
    constexpr int left = (28 - 1 - s) < (PD - 1) ? (28 - 1 - s) : (PD - 1);
    asm volatile("s_waitcnt vmcnt(%4)" : "+v"(hb[b][0]), "+v"(hb[b][1]), "+v"(wb[b][0]), "+v"(wb[b][1]) : "n"(4 * left));
    const bf8 h0 = __builtin_bit_cast(bf8, hb[b][0]), h1 = __builtin_bit_cast(bf8, hb[b][1]);
    const bf8 w0 = __builtin_bit_cast(bf8, wb[b][0]), w1 = __builtin_bit_cast(bf8, wb[b][1]);
    acc[0][0] = __builtin_amdgcn_mfma_f32_32x32x16_bf16(w0, h0, acc[0][0], 0, 0, 0);
    acc[0][1] = __builtin_amdgcn_mfma_f32_32x32x16_bf16(w0, h1, acc[0][1], 0, 0, 0);
    acc[1][0] = __builtin_amdgcn_mfma_f32_32x32x16_bf16(w1, h0, acc[1][0], 0, 0, 0);
    acc[1][1] = __builtin_amdgcn_mfma_f32_32x32x16_bf16(w1, h1, acc[1][1], 0, 0, 0);
    __builtin_amdgcn_sched_barrier(0);
    if constexpr (s + PD < 28) ldstep(s + PD);
    __builtin_amdgcn_sched_barrier(0);
  });
  u4 xr0 = *(const u4*)(x + oidx), xr1 = *(const u4*)(x + oidx + 8);
  float* red = (float*)smem;
  auto put = [&](int a, int b) {
#pragma unroll
    for (int g = 0; g < 4; ++g) {
      f4 v = {acc[a][b][4 * g], acc[a][b][4 * g + 1], acc[a][b][4 * g + 2], acc[a][b][4 * g + 3]};
      *(f4*)(red + (w * 64 + b * 32 + r32) * 68 + a * 32 + 8 * g + 4 * hh) = v;
    }
  };
  STAMP(2);
#pragma unroll
  for (int a = 0; a < 2; ++a)
#pragma unroll
    for (int b = 0; b < 2; ++b)
      if (w != 2 * a + b) put(a, b);
  {
    BAR();
    STAMP(3);
    const int dt = w >> 1, tt = w & 1;
    const int yo = ((nt * 64 + dt * 32 + r32) * 2048 + mt * 64 + tt * 32 + 8 * hh) * 2;
    u4 y0, y1;
    for (unsigned spins = 0; spins < 4000000u; ++spins) {
      y0 = __builtin_amdgcn_raw_buffer_load_b128(yrs, yo, 0, 16);
      y1 = __builtin_amdgcn_raw_buffer_load_b128(yrs, yo + 32, 0, 16);
      bool bad = false;
#pragma unroll
      for (int p = 0; p < 4; ++p) bad = bad || y0[p] == 0xffffffffu || y1[p] == 0xffffffffu;
      if (__builtin_amdgcn_ballot_w64(bad) == 0ull) break;
      __builtin_amdgcn_s_sleep(2);
      asm volatile("" ::: "memory");
    }
    u4 i0, i1;
#pragma unroll
    for (int p = 0; p < 4; ++p) {
      int k = 8 * hh + 2 * p;
      i0[p] = (k == r32 ? 0x3f80u : 0u) | (k + 1 == r32 ? 0x3f800000u : 0u);
      i1[p] = (k + 16 == r32 ? 0x3f80u : 0u) | (k + 17 == r32 ? 0x3f800000u : 0u);
    }
    f16v t;
#pragma unroll
    for (int e = 0; e < 16; ++e) t[e] = 0.f;
    t = __builtin_amdgcn_mfma_f32_32x32x16_bf16(__builtin_bit_cast(bf8, y0), __builtin_bit_cast(bf8, i0), t, 0, 0, 0);
    t = __builtin_amdgcn_mfma_f32_32x32x16_bf16(__builtin_bit_cast(bf8, y1), __builtin_bit_cast(bf8, i1), t, 0, 0, 0);
#pragma unroll
    for (int a = 0; a < 2; ++a)
#pragma unroll
      for (int b = 0; b < 2; ++b)
        if (w == 2 * a + b) { acc[a][b] += t; put(a, b); }
  }
  STAMP(4); WAITLGKM(); BAR(); STAMP(5);
  u4 o[2];
#pragma unroll
  for (int q = 0; q < 4; ++q) {
    const float* rp = red + tok * 68 + dc + 4 * q;
    f4 v = (*(const f4*)rp + *(const f4*)(rp + 64 * 68)) + (*(const f4*)(rp + 128 * 68) + *(const f4*)(rp + 192 * 68));
    unsigned xa = q < 2 ? xr0[2 * q] : xr1[2 * q - 4], xb = q < 2 ? xr0[2 * q + 1] : xr1[2 * q - 3];
    o[q >> 1][2 * (q & 1)] = pk_bf(v[0] + bf_lo(xa), v[1] + bf_hi(xa));
    o[q >> 1][2 * (q & 1) + 1] = pk_bf(v[2] + bf_lo(xb), v[3] + bf_hi(xb));
  }
  *(u4*)(out + oidx) = o[0];
  *(u4*)(out + oidx + 8) = o[1];
  STAMP(6); STAMP_OUT(1);
}

extern "C" void kernel_launch(void* const* d_in, const int* in_sizes, int n_in, void* d_out, int out_size, void* d_ws,
                              size_t ws_size, hipStream_t stream) {
  (void)in_sizes; (void)n_in; (void)out_size; (void)ws_size;
  typedef const unsigned short* cp;
  (void)hipFuncSetAttribute((const void*)k1_kernel, hipFuncAttributeMaxDynamicSharedMemorySize, K1_LDS);
  (void)hipFuncSetAttribute((const void*)k2_kernel, hipFuncAttributeMaxDynamicSharedMemorySize, K2_LDS);
  k1_kernel<<<256, 256, K1_LDS, stream>>>((cp)d_in[0], (cp)d_in[8], (cp)d_in[9], (cp)d_in[10], (char*)d_ws);
  k2_kernel<<<256, 512, K2_LDS, stream>>>((cp)d_in[0], (cp)d_in[1], (cp)d_in[2], (cp)d_in[3], (cp)d_in[4], (cp)d_in[5],
                                          (cp)d_in[6], (char*)d_ws, (unsigned short*)d_out);
}
```

```cpp
#include <hip/hip_runtime.h>
#include <hip/hip_bf16.h>
#include <stdint.h>

typedef __attribute__((ext_vector_type(2))) __bf16 bf2;
typedef __attribute__((ext_vector_type(8))) __bf16 bf8;
typedef __attribute__((ext_vector_type(2))) _Float16 h2;
typedef __attribute__((ext_vector_type(8))) _Float16 h8;
typedef __attribute__((ext_vector_type(2))) float f2;
typedef __attribute__((ext_vector_type(4))) float f4;
typedef __attribute__((ext_vector_type(16))) float f16v;
typedef __attribute__((ext_vector_type(2))) unsigned u2;
typedef __attribute__((ext_vector_type(4))) unsigned u4;
typedef __attribute__((ext_vector_type(4))) short s4;
#define LDSP __attribute__((address_space(3)))
#define DEVI __device__ __forceinline__

constexpr int SEQ = 2048, DM = 512, FF = 1792;
constexpr int XT_ROW = 4608;
constexpr size_t OFF_H = 0;
constexpr size_t OFF_WF = OFF_H + (size_t)64 * 112 * 1024;
constexpr size_t OFF_XT = OFF_WF + (size_t)16 * 112 * 1024;
constexpr size_t OFF_YA = OFF_XT + (size_t)512 * XT_ROW + 1024;
constexpr size_t OFF_CNT = OFF_YA + (size_t)512 * 2048 * 2;

DEVI float bf_lo(unsigned v) { return __builtin_bit_cast(float, v << 16); }
DEVI float bf_hi(unsigned v) { return __builtin_bit_cast(float, v & 0xffff0000u); }
DEVI unsigned pk_bf(float a, float b) { f2 f = {a, b}; return __builtin_bit_cast(unsigned, __builtin_convertvector(f, bf2)); }
DEVI unsigned pk_h(float a, float b) { f2 f = {a, b}; return __builtin_bit_cast(unsigned, __builtin_convertvector(f, h2)); }
DEVI float rbf(float a) { return bf_lo(pk_bf(a, 0.f)); }
DEVI float ldbf(const unsigned short* p) { return __builtin_bit_cast(float, ((unsigned)*p) << 16); }
DEVI float dot2(unsigned a, unsigned b, float c) {
  return __builtin_amdgcn_fdot2_f32_bf16(__builtin_bit_cast(bf2, a), __builtin_bit_cast(bf2, b), c, false);
}
DEVI void glds16(const void* g, unsigned lds_base) {
  unsigned sv;
  asm volatile("s_mov_b32 %0, m0\n\ts_mov_b32 m0, %2\n\ts_nop 0\n\tglobal_load_lds_dwordx4 %1, off\n\ts_mov_b32 m0, %0"
               : "=&s"(sv) : "v"(g), "s"(lds_base) : "memory");
}
DEVI void glds16s(unsigned voff, const void* sbase, unsigned lds_base) {
  asm volatile("s_mov_b32 m0, %2\n\ts_nop 0\n\tglobal_load_lds_dwordx4 %0, %1" :: "v"(voff), "s"(sbase), "s"(lds_base) : "memory", "m0");
}
#define WAITVM(n) asm volatile("s_waitcnt vmcnt(" #n ")" ::: "memory")
#define WAITLGKM() asm volatile("s_waitcnt lgkmcnt(0)" ::: "memory")
#define BAR() do { asm volatile("" ::: "memory"); __builtin_amdgcn_s_barrier(); asm volatile("" ::: "memory"); } while (0)
#define GLD(dst, voff, sptr, imm) asm volatile("global_load_dwordx4 %0, %1, %2 offset:" #imm : "=v"(dst) : "v"(voff), "s"(sptr))
#define GLDH(dst, voff, sptr) asm volatile("global_load_ushort %0, %1, %2" : "=v"(dst) : "v"(voff), "s"(sptr))
#define SB() __builtin_amdgcn_sched_barrier(0)
template <int V> struct ic { static constexpr int value = V; };
template <int I, int N, class F> DEVI void static_for(F&& f) { if constexpr (I < N) { f(ic<I>{}); static_for<I + 1, N>(f); } }
template <int N> DEVI void waitvm() { asm volatile("s_waitcnt vmcnt(%0)" ::"n"(N) : "memory"); }

#define STAMP(i)
#define STAMP_DECL
#define STAMP_OUT(k)
struct cf { float r, i; };
DEVI cf cmul(cf a, cf b) { cf c; c.r = a.r * b.r - a.i * b.i; c.i = a.r * b.i + a.i * b.r; return c; }
DEVI cf zpow(float a2, float rev, float t) {
  float e = __builtin_amdgcn_exp2f(a2 * t);
  float p = __builtin_amdgcn_fractf(rev * t);
  cf c; c.r = e * __builtin_amdgcn_cosf(p); c.i = e * __builtin_amdgcn_sinf(p); return c;
}
DEVI float wave_sum(float v) {
#pragma unroll
  for (int o = 32; o >= 1; o >>= 1) v += __shfl_xor(v, o);
  return v;
}

constexpr int K1_LDS = 10 * 16384;

DEVI void kgen_wave(char* sa, char* sb, int lane, float a2, float rev, cf C) {
  char* wq = sa + lane * 4; char* w0 = wq + 4352; char* w1 = sb + lane * 4; char* w2 = w1 + 4352;
  float* tab = (float*)(sa + 64);
  const int rd = (lane & 15) * 272 + (lane >> 4) * 16;
  cf z1 = zpow(a2, rev, 1.f), z16 = zpow(a2, rev, 16.f), z256 = zpow(a2, rev, 256.f), z1024 = zpow(a2, rev, 1024.f);
  const cf z32 = cmul(z16, z16), z2 = cmul(z1, z1), z512 = cmul(z256, z256), z768 = cmul(z512, z256);
  cf wz[4]; wz[0] = z1024; wz[1] = cmul(z1024, z256); wz[2] = cmul(z1024, z512); wz[3] = cmul(z1024, z768);
  cf e = {1.f, 0.f}, o = z16, pe = C, po = cmul(C, z1);
  { cf p = cmul(cmul(C, z1024), z1024); *(unsigned*)(w2 + 8 * 272) = pk_h(p.r, p.i); }
#pragma unroll
  for (int r = 0; r < 8; ++r) {
    *(unsigned*)(wq + (2 * r) * 272) = pk_h(e.r, -e.i);
    *(unsigned*)(wq + (2 * r + 1) * 272) = pk_h(o.r, -o.i);
    *(unsigned*)(w0 + (2 * r) * 272) = pk_h(pe.r, pe.i);
    *(unsigned*)(w0 + (2 * r + 1) * 272) = pk_h(po.r, po.i);
    { cf p = cmul(pe, z256); *(unsigned*)(w1 + r * 272) = pk_h(p.r, p.i); }
    if ((r & 1) == 0) {
      cf p = cmul(pe, z512), q = cmul(pe, z768);
      *(unsigned*)(w1 + (8 + (r >> 1)) * 272) = pk_h(p.r, p.i); *(unsigned*)(w1 + (12 + (r >> 1)) * 272) = pk_h(q.r, q.i);
    }
    if ((r & 3) == 0) {
#pragma unroll
      for (int f = 0; f < 4; ++f) { cf p = cmul(pe, wz[f]); *(unsigned*)(w2 + (2 * f + (r >> 2)) * 272) = pk_h(p.r, p.i); }
    }
    if (r < 7) { e = cmul(e, z32); o = cmul(o, z32); pe = cmul(pe, z2); po = cmul(po, z2); }
  }
  f4 a0 = {0.f, 0.f, 0.f, 0.f}, a1 = a0, a2v = a0;
#pragma unroll
  for (int s = 0; s < 4; ++s) {
    const h8 qf = *(const h8*)(sa + rd + s * 64);
    a0 = __builtin_amdgcn_mfma_f32_16x16x32_f16(*(const h8*)(sa + 4352 + rd + s * 64), qf, a0, 0, 0, 0);
    a1 = __builtin_amdgcn_mfma_f32_16x16x32_f16(*(const h8*)(sb + rd + s * 64), qf, a1, 0, 0, 0);
    a2v = __builtin_amdgcn_mfma_f32_16x16x32_f16(*(const h8*)(sb + 4352 + rd + s * 64), qf, a2v, 0, 0, 0);
  }
  const int col = lane & 15, rq = lane >> 4;
  *(f4*)(tab + 4 * rq + 16 * col) = a0;
  *(f4*)(tab + ((rq < 2) ? (256 + 4 * rq + 8 * col) : (384 + 64 * (rq & 1) + 4 * col))) = a1;
  if (rq < 2) {
    *(f2*)(tab + 512 + 64 * rq + 2 * col) = f2{a2v[0], a2v[1]};
    *(f2*)(tab + 512 + 64 * rq + 32 + 2 * col) = f2{a2v[2], a2v[3]};
  } else if (lane == 32) {
    tab[640] = a2v[0];
  }
}

DEVI void kput(char* kc, int blk, const float* v) {
  unsigned E[5], O[5];
#pragma unroll
  for (int k = 0; k < 5; ++k) { E[k] = pk_h(v[2 * k], v[2 * k + 1]); O[k] = pk_h(v[2 * k + 1], v[2 * k + 2]); }
  *(u4*)(kc + 16 * blk) = u4{E[0], E[1], E[2], E[3]};
  *(u4*)(kc + 4160 + 16 * blk) = u4{O[0], O[1], O[2], O[3]};
  *(u4*)(kc + 2 * 4160 + 16 * blk) = u4{E[1], E[2], E[3], E[4]};
  *(u4*)(kc + 3 * 4160 + 16 * blk) = u4{O[1], O[2], O[3], O[4]};
}
DEVI void kexpand(const float* tA, const float* tB, int t, int par, char* kc) {
  float v[11];
  {
    const int m = 255 - t;
    float z = tA[383 + m] + tB[383 + m], a = tA[384 + m] + tB[384 + m], b = tA[385 + m] + tB[385 + m];
    v[0] = b; v[1] = b; v[2] = b; v[3] = (m & 1) ? b : a; v[4] = a; v[5] = a; v[6] = a; v[7] = a; v[8] = a; v[9] = a;
    v[10] = m == 128 ? z : a;
    kput(kc, t, v);
  }
  const int m = 127 - t;
  if (par == 0) {
    const int bs = 256 + 2 * m;
    float y = tA[bs - 2] + tB[bs - 2], z = tA[bs - 1] + tB[bs - 1], a = tA[bs] + tB[bs], b = tA[bs + 1] + tB[bs + 1], c = tA[bs + 2] + tB[bs + 2];
    v[0] = c; v[1] = c; v[2] = b; v[3] = b; v[4] = b; v[5] = a; v[6] = a; v[7] = a; v[8] = a;
    v[9] = m == 64 ? z : a; v[10] = m == 64 ? y : z;
  } else {
    const bool s0 = m < 32, s1 = m <= 32, nz = m != 0;
    const int bs = s0 ? 8 * m - 3 : 125 + 4 * m;
    float W[11];
#pragma unroll
    for (int i = 0; i < 11; ++i) W[i] = tA[bs + i] + tB[bs + i];
    v[0] = s0 ? W[10] : W[7]; v[1] = s0 ? W[9] : W[6]; v[2] = s0 ? W[8] : W[5]; v[3] = s0 ? W[7] : W[5];
    v[4] = s0 ? W[6] : W[5]; v[5] = s0 ? W[5] : W[4]; v[6] = s0 ? W[4] : W[3]; v[7] = W[3];
    v[8] = nz ? (s1 ? W[2] : W[3]) : 0.f; v[9] = nz ? (s1 ? W[1] : W[2]) : 0.f; v[10] = nz ? (s1 ? W[0] : W[1]) : 0.f;
  }
  kput(kc, 128 + t, v);
}

DEVI bf8 tr_frag(unsigned addr) {
  s4 a = __builtin_amdgcn_ds_read_tr16_b64_v4i16((LDSP s4*)(uintptr_t)addr);
  s4 b = __builtin_amdgcn_ds_read_tr16_b64_v4i16((LDSP s4*)(uintptr_t)(addr + 256));
  typedef __attribute__((ext_vector_type(8))) short s8;
  s8 r = __builtin_shufflevector(a, b, 0, 1, 2, 3, 4, 5, 6, 7);
  return __builtin_bit_cast(bf8, r);
}
DEVI float ln_inv(float var) { return 1.f / rbf(__builtin_sqrtf(rbf(rbf(fmaxf(var, 0.f)) + 1e-5f))); }

DEVI void warm(const void* a0, const void* a1, const void* a2, const void* a3, const void* b0, const void* b1, const void* b2,
               unsigned lds0) {
  const unsigned t = threadIdx.x, bid = blockIdx.x;
  const unsigned w = __builtin_amdgcn_readfirstlane(t >> 6);
  const uintptr_t x0 = (w & 1) ? (uintptr_t)a1 : (uintptr_t)a0, x1 = (w & 1) ? (uintptr_t)a3 : (uintptr_t)a2;
  const uintptr_t y0 = (w & 1) ? (uintptr_t)b1 : (uintptr_t)b0;
  const uintptr_t pa = ((w & 2) ? x1 : x0) + bid * 512u;
  const uintptr_t pb = (w & 2) ? (uintptr_t)b2 + (bid * 8192u + (w & 1) * 4096u) : y0 + ((bid * 4u) & ~15u);
  const uintptr_t q = (t & 32) ? pb : pa + (t & 31) * 16u;
  glds16((const void*)q, lds0 + 5 * 16384 + w * 1024);
}

constexpr int slot_of(int s) { return (s + 8) % 10; }

__global__ void __launch_bounds__(256, 1)
k1_kernel(const unsigned short* __restrict__ x, const unsigned short* __restrict__ wg, const unsigned short* __restrict__ wu,
          const unsigned short* __restrict__ wd, char* __restrict__ ws, const unsigned short* __restrict__ logA,
          const unsigned short* __restrict__ Aim, const unsigned short* __restrict__ Cre, const unsigned short* __restrict__ Cim,
          const unsigned short* __restrict__ logdt, const unsigned short* __restrict__ dskip, const unsigned short* out) {
  extern __shared__ __attribute__((aligned(16))) char smem[];
  const int tid = threadIdx.x, lane = tid & 63;
  const int w = __builtin_amdgcn_readfirstlane(tid >> 6);
  const int bid = blockIdx.x, xcd = bid & 7, idx = bid >> 3;
  const int mt = 4 * (xcd >> 1) + idx / 7, ft = 7 * (xcd & 1) + idx % 7;
  const unsigned lds0 = (unsigned)(uintptr_t)(LDSP char*)smem;
  const int r32 = lane & 31, hh = lane >> 5;
  STAMP_DECL; STAMP(0);
  {
    const u4 m = {~0u, ~0u, ~0u, ~0u};
    *(u4*)(ws + OFF_YA + bid * 8192 + tid * 16) = m; *(u4*)(ws + OFF_YA + bid * 8192 + 4096 + tid * 16) = m;
  }

  if (idx >= 28) {
    const int sp = xcd * 4 + (idx - 28);
    {
      const unsigned short* xp = x + (size_t)(sp * 64 + lane) * DM + w * 128;
      u4 xv[16];
#pragma unroll
      for (int j = 0; j < 16; ++j) xv[j] = *(const u4*)(xp + 8 * j);
      float s0 = 0.f, s1 = 0.f, q0 = 0.f, q1 = 0.f;
#pragma unroll
      for (int j = 0; j < 16; ++j)
#pragma unroll
        for (int p = 0; p < 4; p += 2) {
          s0 = dot2(xv[j][p], 0x3f803f80u, s0); s1 = dot2(xv[j][p + 1], 0x3f803f80u, s1);
          q0 = dot2(xv[j][p], xv[j][p], q0); q1 = dot2(xv[j][p + 1], xv[j][p + 1], q1);
        }
      f2* st2 = (f2*)smem;
      st2[w * 64 + lane] = f2{s0 + s1, q0 + q1};
      STAMP(3);
      __syncthreads();
      const f2 tt = (st2[lane] + st2[64 + lane]) + (st2[128 + lane] + st2[192 + lane]);
      const float mean = tt[0] * (1.f / 512.f), mb = rbf(mean);
      const float inv = ln_inv(tt[1] * (1.f / 512.f) - mean * mean);
      char* xd = ws + OFF_XT + (size_t)(w * 128) * XT_ROW + 144 * sp;
      const unsigned xo = (lane & 1) * XT_ROW + (lane >> 1) * 4, sel = (lane & 1) ? 0x03020706u : 0x05040100u;
#pragma unroll
      for (int j = 0; j < 16; ++j)
#pragma unroll
        for (int p = 0; p < 4; ++p) {
          unsigned d = pk_bf(bf_lo(xv[j][p]) - mb, bf_hi(xv[j][p]) - mb);
          unsigned n = pk_bf(bf_lo(d) * inv, bf_hi(d) * inv);
          unsigned hv = pk_h(bf_lo(n), bf_hi(n));
          unsigned ot = __builtin_amdgcn_update_dpp(0u, hv, 0xB1, 0xf, 0xf, false);
          *(unsigned*)(xd + (size_t)(8 * j + 2 * p) * XT_ROW + xo) = __builtin_amdgcn_perm(ot, hv, sel);
        }
    }
    STAMP(1);
    warm(logA, Aim, Cre, Cim, logdt, dskip, out, lds0);
    const int fr0 = (sp * 4 + w) * 14;
#pragma unroll 1
    for (int b = 0; b < 2; ++b) {
      unsigned short v[7][8];
      const int fr = fr0 + b * 7, nt = fr / 112, ks = fr % 112;
      const unsigned short* p = wd + (size_t)(16 * ks + 4 * hh) * DM + 32 * nt + r32;
#pragma unroll
      for (int i = 0; i < 7; ++i)
#pragma unroll
        for (int j = 0; j < 8; ++j) v[i][j] = p[(16 * i + 8 * (j >> 2) + (j & 3)) * DM];
#pragma unroll
      for (int i = 0; i < 7; ++i) {
        u4 o;
#pragma unroll
        for (int j = 0; j < 4; ++j) o[j] = (unsigned)v[i][2 * j] | ((unsigned)v[i][2 * j + 1] << 16);
        *(u4*)(ws + OFF_WF + (size_t)(fr + i) * 1024 + lane * 16) = o;
      }
    }
    STAMP(2); STAMP_OUT(0);
    WAITVM(0);
    return;
  }

  const unsigned wvo = ((8 * w + ((lane >> 2) & 7)) * FF + ft * 128 + (lane >> 5) * 32 + (lane & 3) * 8) * 2;
  const unsigned xsl = lds0 + w * 4096;
  auto issue_piece = [&](int s, int q) {
    glds16s(wvo, (q & 2 ? wu : wg) + (size_t)s * 32 * FF + (q & 1) * 64, xsl + slot_of(s) * 16384 + q * 1024);
  };
  auto issue_slab = [&](int s) { issue_piece(s, 0); issue_piece(s, 1); issue_piece(s, 2); issue_piece(s, 3); };
  u4 xf[32];
  float mean, var;
  {
    unsigned xvo[4];
#pragma unroll
    for (int j = 0; j < 4; ++j) xvo[j] = (mt * 128 + w * 32 + 4 * j + (lane >> 4)) * 1024 + ((((lane & 15) ^ (lane >> 4)) ^ (4 * j)) * 16);
    auto issue_x = [&](int c) {
#pragma unroll
      for (int j = 0; j < 8; ++j) glds16s(xvo[j & 3], x + 128 * c + (j >> 2) * 8192, xsl + c * 32768 + (j >> 2) * 16384 + 1024 * (j & 3));
    };
    issue_x(0); issue_x(1); issue_x(2);
    unsigned xb0 = xsl + (r32 >> 4) * 16384 + (r32 & 15) * 256, xb1 = xb0 + 65536;
    asm volatile("" : "+v"(xb0), "+v"(xb1));
    float s0 = 0.f, s1 = 0.f, q0 = 0.f, q1 = 0.f;
    static_for<0, 4>([&](auto H) {
      constexpr int c = H.value;
      waitvm<16>();
#pragma unroll
      for (int s = 0; s < 8; ++s)
        xf[8 * c + s] = *(const LDSP u4*)(uintptr_t)(((c & 2) ? xb1 : xb0) + (((2 * s + hh) ^ (r32 & 15)) * 16) + (c & 1) * 32768);
      if constexpr (c == 0) issue_x(3);
      if constexpr (c == 1) { issue_slab(0); issue_slab(1); }
#pragma unroll
      for (int s = 8 * c; s < 8 * c + 8; ++s)
#pragma unroll
        for (int p = 0; p < 4; p += 2) {
          s0 = dot2(xf[s][p], 0x3f803f80u, s0); s1 = dot2(xf[s][p + 1], 0x3f803f80u, s1);
          q0 = dot2(xf[s][p], xf[s][p], q0); q1 = dot2(xf[s][p + 1], xf[s][p + 1], q1);
        }
      asm volatile("" ::"v"(s0), "v"(s1), "v"(q0), "v"(q1) : "memory");
      if constexpr (c == 2) { issue_slab(2); issue_slab(3); }
      if constexpr (c == 3) issue_slab(4);
    });
    STAMP(1);
    float t = s0 + s1, q = q0 + q1;
    t += __shfl_xor(t, 32); q += __shfl_xor(q, 32);
    mean = t * (1.f / 512.f);
    var = q * (1.f / 512.f) - mean * mean;
  }
  STAMP(2);
  const float mb = rbf(mean);
  auto center = [&](int s, int p) { xf[s][p] = pk_bf(bf_lo(xf[s][p]) - mb, bf_hi(xf[s][p]) - mb); };
#pragma unroll
  for (int p = 0; p < 8; ++p) center(p >> 2, p & 3);
  f16v acc[8];
#pragma unroll
  for (int c = 0; c < 8; ++c)
#pragma unroll
    for (int e = 0; e < 16; ++e) acc[c][e] = 0.f;
  unsigned tb[3];
  tb[0] = lds0 + hh * 4096 + ((lane & 15) >> 2) * 64 + ((lane >> 4) & 1) * 32 + (lane & 3) * 8;
  tb[1] = tb[0] + 65536; tb[2] = tb[0] + 131072;
  asm volatile("" : "+v"(tb[0]), "+v"(tb[1]), "+v"(tb[2]));
  auto wfrag = [&](int n, int c) {
    const int sl = slot_of(n >> 2), nG = n & 3;
    return tr_frag(tb[sl >> 2] + (sl & 3) * 16384 + (nG >> 1) * 8192 + (nG & 1) * 2048 + c * 512);
  };
  waitvm<16>();
  BAR();
  bf8 wf[3][4];
#pragma unroll
  for (int c = 0; c < 8; ++c) wf[c >> 2][c & 3] = wfrag(c >> 2, c & 3);
  STAMP(4);
  SB();
  static_for<0, 64>([&](auto GG) {
    constexpr int g = GG.value, i = g >> 2, G = g & 3;
    if constexpr (G == 1 && i < 15) {
      waitvm<(i <= 11 ? 12 : 4 * (14 - i))>();
      BAR();
    }
    __builtin_amdgcn_s_waitcnt(g < 63 ? 0xC87F : 0xC07F);
    SB();
    const bf8 xb = __builtin_bit_cast(bf8, xf[2 * i + (G >> 1)]);
#pragma unroll
    for (int c = 0; c < 4; ++c) {
      acc[(G & 1) * 4 + c] = __builtin_amdgcn_mfma_f32_32x32x16_bf16(wf[g % 3][c], xb, acc[(G & 1) * 4 + c], 0, 0, 0);
      if constexpr (g + 2 < 64) wf[(g + 2) % 3][c] = wfrag(g + 2, c);
      if constexpr (g >= 1 && (g - 1) / 4 + 5 <= 15) { if (c == 1) issue_piece((g - 1) / 4 + 5, (g - 1) & 3); }
      if constexpr (i < 15) { if ((c & 1) == 0) center(2 * i + 2 + (G >> 1), (G & 1) * 2 + (c >> 1)); }
      SB();
    }
  });

  STAMP(5);
  warm(logA, Aim, Cre, Cim, logdt, dskip, out, lds0);
  const float inv = ln_inv(var);
  const float c1 = -inv * 1.44269504088896f, c2 = inv * inv;
  char* hdst = ws + OFF_H + ((size_t)(mt * 4 + w) * 112 + ft * 8) * 1024 + lane * 16;
#pragma unroll
  for (int c = 0; c < 4; ++c) {
    unsigned o[8];
#pragma unroll
    for (int p = 0; p < 8; ++p) {
      f2 g = f2{acc[c][2 * p], acc[c][2 * p + 1]};
      f2 u = f2{acc[4 + c][2 * p], acc[4 + c][2 * p + 1]};
      f2 t = g * c1;
      f2 ex = f2{__builtin_amdgcn_exp2f(t[0]), __builtin_amdgcn_exp2f(t[1])} + 1.f;
      f2 r = f2{__builtin_amdgcn_rcpf(ex[0]), __builtin_amdgcn_rcpf(ex[1])};
      f2 hv = (g * u) * (r * c2);
      o[p] = pk_bf(hv[0], hv[1]);
    }
    u4 a = {o[0], o[1], o[2], o[3]}, b = {o[4], o[5], o[6], o[7]};
    *(u4*)(hdst + (2 * c) * 1024) = a;
    *(u4*)(hdst + (2 * c + 1) * 1024) = b;
    SB();
  }
  STAMP(6); STAMP_OUT(0);
  WAITVM(0);
}

constexpr int K2_RED = 4 * 64 * 272;
constexpr int C_XS = 0, C_K = 7424, C_Y = 7424 + 4 * 4160, C_SZ = C_Y + 20480;
constexpr int K2_SYNC = K2_RED + 2 * C_SZ;
constexpr int K2_LDS = K2_SYNC + 64;

DEVI void pair_sync(LDSP unsigned* c, unsigned target, int lane) {
  asm volatile("s_waitcnt vmcnt(0) lgkmcnt(0)" ::: "memory");
  if (lane == 0) __hip_atomic_fetch_add(c, 1u, __ATOMIC_RELAXED, __HIP_MEMORY_SCOPE_WORKGROUP);
  while (__hip_atomic_load(c, __ATOMIC_RELAXED, __HIP_MEMORY_SCOPE_WORKGROUP) < target) __builtin_amdgcn_s_sleep(1);
  asm volatile("" ::: "memory");
}
DEVI h8 kfrag(const LDSP char* p, const LDSP char* p8, int off) {
  u2 a = *(const LDSP u2*)(p + off), b = *(const LDSP u2*)(p8 + off);
  u4 r = {a[0], a[1], b[0], b[1]};
  return __builtin_bit_cast(h8, r);
}
constexpr int xoff(int v) { return 32 * v + 16 * (v >> 2); }

template <int U0, int U1>
DEVI void conv_near(const LDSP char* xa, const LDSP char* kb, const LDSP char* kb8, f16v& a0, f16v& a1) {
  constexpr int D = 3;
  h8 xq[D + 2], kq[D];
#pragma unroll
  for (int i = 0; i < D + 2; ++i) xq[i] = *(const LDSP h8*)(xa + xoff(U0 + i - 64));
#pragma unroll
  for (int i = 0; i < D; ++i) kq[i] = kfrag(kb, kb8, 32 * (U0 + i));
  SB();
  static_for<U0, U1>([&](auto U) {
    constexpr int u = U.value, i = u - U0;
    a0 = __builtin_amdgcn_mfma_f32_32x32x16_f16(xq[i % (D + 2)], kq[i % D], a0, 0, 0, 0);
    if constexpr (u + D < U1) xq[i % (D + 2)] = *(const LDSP h8*)(xa + xoff(u + D + 2 - 64));
    SB();
    a1 = __builtin_amdgcn_mfma_f32_32x32x16_f16(xq[(i + 2) % (D + 2)], kq[i % D], a1, 0, 0, 0);
    if constexpr (u + D < U1) kq[i % D] = kfrag(kb, kb8, 32 * (u + D));
    SB();
  });
}

__global__ void __launch_bounds__(512, 2)
k2_kernel(const unsigned short* __restrict__ x, const unsigned short* __restrict__ logA, const unsigned short* __restrict__ Aim,
          const unsigned short* __restrict__ Cre, const unsigned short* __restrict__ Cim, const unsigned short* __restrict__ logdt,
          const unsigned short* __restrict__ dskip, char* __restrict__ ws,
          unsigned short* __restrict__ out) {
  extern __shared__ __attribute__((aligned(16))) char smem[];
  const int tid = threadIdx.x, lane = tid & 63;
  const int w = __builtin_amdgcn_readfirstlane(tid >> 6);
  const int bid = blockIdx.x, xcd = bid & 7, idx = bid >> 3;
  const unsigned lds0 = (unsigned)(uintptr_t)(LDSP char*)smem;
  const int r32 = lane & 31, hh = lane >> 5;
  LDSP unsigned* sy = (LDSP unsigned*)(uintptr_t)(lds0 + K2_SYNC);
  auto yrs = __builtin_amdgcn_make_buffer_rsrc((void*)(ws + OFF_YA), (short)0, 512 * 2048 * 2, 0x00020000);

  STAMP_DECL; STAMP(0);
  unsigned pla_ = 0, pai_ = 0, pcr_ = 0, pci_ = 0, qcr_ = 0, qci_ = 0, pld_ = 0, dsk_ = 0;
  if (w >= 4) {
    __builtin_amdgcn_s_setprio(3);
    const unsigned nc = (2 * bid + ((w - 4) >> 1)) * 2, n0 = nc * 128 + ((w & 1) * 64 + lane) * 2, n1 = n0 ^ 128u;
    GLDH(pla_, n0, logA); GLDH(pai_, n0, Aim); GLDH(pcr_, n0, Cre); GLDH(pci_, n0, Cim);
    GLDH(qcr_, n1, Cre); GLDH(qci_, n1, Cim); GLDH(pld_, nc, logdt); GLDH(dsk_, nc, dskip);
  }
  if (w == 4 && lane < 16) sy[lane] = 0u;
  WAITLGKM(); BAR();
  if (w >= 4) {
    const int cl = (w - 4) >> 1, wv = w & 1, ch = 2 * bid + cl;
    char* cb = smem + K2_RED + cl * C_SZ;
    const unsigned cl0 = lds0 + K2_RED + cl * C_SZ;
    {
      const char* xsrc = ws + OFF_XT + (size_t)ch * XT_ROW + lane * 16;
      if (wv == 0) { glds16(xsrc, cl0 + 2304); glds16(xsrc + 1024, cl0 + 3328); glds16(xsrc + 2048, cl0 + 4352); }
      else { glds16(xsrc + 3072, cl0 + 5376); glds16(xsrc + 4096, cl0 + 6400); glds16(xsrc + 2048, cl0 + 4352); }
      u4 z = {0u, 0u, 0u, 0u};
      if (wv == 0) {
        *(u4*)(cb + lane * 16) = z; *(u4*)(cb + 1024 + lane * 16) = z;
        if (lane < 16) *(u4*)(cb + 2048 + lane * 16) = z;
      }
    }
    asm volatile("s_waitcnt vmcnt(3)" : "+v"(pla_), "+v"(pai_), "+v"(pcr_), "+v"(pci_), "+v"(qcr_), "+v"(qci_), "+v"(pld_), "+v"(dsk_));
    const float pla = bf_lo(pla_), pai = bf_lo(pai_), pcr = bf_lo(pcr_), pci = bf_lo(pci_), qcr = bf_lo(qcr_), qci = bf_lo(qci_);
    const float pld = bf_lo(pld_), dsk = bf_lo(dsk_);
    float scale;
    {
      float dt = rbf(__expf(pld));
      float are = -rbf(__expf(pla));
      float a2 = (are * dt) * 1.44269504088896f, rev = (pai * dt) * 0.159154943091895f;
      scale = 2.f * dt * wave_sum(fabsf(pcr) + fabsf(pci) + fabsf(qcr) + fabsf(qci));
      float isc = scale > 0.f ? 2.f * dt / scale : 0.f;
      cf C = {pcr * isc, pci * isc};
      STAMP(5);
      kgen_wave(cb + C_Y + wv * 10240, cb + C_K + wv * 8320, lane, a2, rev, C);
      STAMP(6);
      pair_sync(sy + 6 + cl, 2u, lane);
      const float* tA = (const float*)(cb + C_Y + 64);
      kexpand(tA, tA + 2560, wv * 64 + lane, wv, cb + C_K);
      if (wv == 0 && lane < 16) *(u4*)(cb + C_K + (lane >> 2) * 4160 + 4096 + (lane & 3) * 16) = u4{0u, 0u, 0u, 0u};
    }
    pair_sync(sy + cl, 2u, lane);
    STAMP(1);
    const int cc = (3 - r32) & 3;
    const LDSP char* cbl = (const LDSP char*)(uintptr_t)cl0;
    const LDSP char* kb = cbl + C_K + cc * 4160 + 2 * (8 * hh - r32 - 1 - cc);
    const LDSP char* kb8 = kb + 8;
    asm volatile("" : "+v"(kb8));
    const LDSP char* xa = cbl + 144 * r32 + 16 * hh;
    float* yb = (float*)(cb + C_Y);
    f16v a0, a1;
#pragma unroll
    for (int e = 0; e < 16; ++e) { a0[e] = 0.f; a1[e] = 0.f; }
    if (wv == 0) {
      conv_near<82, 130>(xa, kb, kb8, a0, a1);
    } else {
      const LDSP char* xfa = cbl + 144 * (r32 >> 1) + 16 * hh + 64 * (r32 & 1);
      const LDSP char* xfb = xfa + 16 * (r32 & 1);
      constexpr int D = 4;
      h8 xq[D], kq[D];
#pragma unroll
      for (int i = 0; i < D; ++i) { xq[i] = *(const LDSP h8*)((((2 + i) & 2) ? xfb : xfa) + xoff(2 + i)); kq[i] = kfrag(kb, kb8, 32 * (2 + i)); }
      SB();
      static_for<2, 66>([&](auto U) {
        constexpr int u = U.value, i = (u - 2) % D;
        if constexpr (u & 1) a1 = __builtin_amdgcn_mfma_f32_32x32x16_f16(xq[i], kq[i], a1, 0, 0, 0);
        else a0 = __builtin_amdgcn_mfma_f32_32x32x16_f16(xq[i], kq[i], a0, 0, 0, 0);
        if constexpr (u + D < 66) { xq[i] = *(const LDSP h8*)((((u + D) & 2) ? xfb : xfa) + xoff(u + D)); kq[i] = kfrag(kb, kb8, 32 * (u + D)); }
        SB();
      });
#pragma unroll
      for (int e = 0; e < 16; ++e) { yb[4096 + 32 * ((e & 3) + 8 * (e >> 2) + 4 * hh) + r32] = a0[e] + a1[e]; a0[e] = 0.f; a1[e] = 0.f; }
      conv_near<66, 82>(xa, kb, kb8, a0, a1);
    }
#pragma unroll
    for (int e = 0; e < 16; ++e) {
      int a = (e & 3) + 8 * (e >> 2) + 4 * hh;
      yb[2048 * wv + 64 * a + r32] = a0[e];
      yb[2048 * wv + 64 * a + 32 + r32] = a1[e];
    }
    STAMP(2);
    pair_sync(sy + 2 + cl, 2u, lane);
    STAMP(3);
#pragma unroll
    for (int rnd = 0; rnd < 2; ++rnd) {
      const int l = 1024 * wv + 512 * rnd + 8 * lane;
      f4 p0 = *(const f4*)(yb + l), p1 = *(const f4*)(yb + l + 4);
      p0 += *(const f4*)(yb + 2048 + l); p1 += *(const f4*)(yb + 2048 + l + 4);
      if (wv) { p0 += *(const f4*)(yb + 3072 + l); p1 += *(const f4*)(yb + 3072 + l + 4); }
      h8 xv = *(const h8*)(cb + 2304 + 2 * l + 16 * (l >> 6));
      u4 o;
#pragma unroll
      for (int p = 0; p < 4; ++p) {
        float y0 = p < 2 ? p0[2 * p] : p1[2 * p - 4], y1 = p < 2 ? p0[2 * p + 1] : p1[2 * p - 3];
        unsigned yv = pk_bf(y0 * scale, y1 * scale);
        unsigned sv = pk_bf((float)xv[2 * p] * dsk, (float)xv[2 * p + 1] * dsk);
        o[p] = pk_bf(bf_lo(yv) + bf_lo(sv), bf_hi(yv) + bf_hi(sv));
        o[p] = o[p] == 0xffffffffu ? 0xfffeffffu : o[p];
      }
      __builtin_amdgcn_raw_buffer_store_b128(o, yrs, (ch * 2048 + l) * 2, 0, 16);
    }
    STAMP(4); STAMP_OUT(1);
    return;
  }

  const int mt = 4 * xcd + (idx >> 3), nt = idx & 7;
  const char* hp = ws + OFF_H + ((size_t)(2 * mt) * 112 + 28 * w) * 1024;
  const char* wp = ws + OFF_WF + ((size_t)(2 * nt) * 112 + 28 * w) * 1024;
  const unsigned vo = lane * 16;
  constexpr int PD = 7;
  u4 hb[PD][2], wb[PD][2];
  auto ldstep = [&](int s) {
    const int b = s % PD, hi = (s >> 2) * 4096;
    switch (s & 3) {
      case 0: GLD(hb[b][0], vo, hp + hi, 0); GLD(wb[b][0], vo, wp + hi, 0); GLD(hb[b][1], vo, hp + 112 * 1024 + hi, 0); GLD(wb[b][1], vo, wp + 112 * 1024 + hi, 0); break;
      case 1: GLD(hb[b][0], vo, hp + hi, 1024); GLD(wb[b][0], vo, wp + hi, 1024); GLD(hb[b][1], vo, hp + 112 * 1024 + hi, 1024); GLD(wb[b][1], vo, wp + 112 * 1024 + hi, 1024); break;
      case 2: GLD(hb[b][0], vo, hp + hi, 2048); GLD(wb[b][0], vo, wp + hi, 2048); GLD(hb[b][1], vo, hp + 112 * 1024 + hi, 2048); GLD(wb[b][1], vo, wp + 112 * 1024 + hi, 2048); break;
      default: GLD(hb[b][0], vo, hp + hi, 3072); GLD(wb[b][0], vo, wp + hi, 3072); GLD(hb[b][1], vo, hp + 112 * 1024 + hi, 3072); GLD(wb[b][1], vo, wp + 112 * 1024 + hi, 3072); break;
    }
  };
  __builtin_amdgcn_s_sleep(8);
#pragma unroll
  for (int s = 0; s < PD; ++s) ldstep(s);
  const int tok = tid >> 2, dc = (tid & 3) * 16;
  const size_t oidx = (size_t)(mt * 64 + tok) * DM + nt * 64 + dc;
  STAMP(1);
  f16v acc[2][2];
#pragma unroll
  for (int e = 0; e < 16; ++e) { acc[0][0][e] = 0.f; acc[0][1][e] = 0.f; acc[1][0][e] = 0.f; acc[1][1][e] = 0.f; }
  static_for<0, 28>([&](auto S) {
    constexpr int s = S.value, b = s % PD;
    constexpr int left = (28 - 1 - s) < (PD - 1) ? (28 - 1 - s) : (PD - 1);
    asm volatile("s_waitcnt vmcnt(%4)" : "+v"(hb[b][0]), "+v"(hb[b][1]), "+v"(wb[b][0]), "+v"(wb[b][1]) : "n"(4 * left));
    const bf8 h0 = __builtin_bit_cast(bf8, hb[b][0]), h1 = __builtin_bit_cast(bf8, hb[b][1]);
    const bf8 w0 = __builtin_bit_cast(bf8, wb[b][0]), w1 = __builtin_bit_cast(bf8, wb[b][1]);
    acc[0][0] = __builtin_amdgcn_mfma_f32_32x32x16_bf16(w0, h0, acc[0][0], 0, 0, 0);
    acc[0][1] = __builtin_amdgcn_mfma_f32_32x32x16_bf16(w0, h1, acc[0][1], 0, 0, 0);
    acc[1][0] = __builtin_amdgcn_mfma_f32_32x32x16_bf16(w1, h0, acc[1][0], 0, 0, 0);
    acc[1][1] = __builtin_amdgcn_mfma_f32_32x32x16_bf16(w1, h1, acc[1][1], 0, 0, 0);
    __builtin_amdgcn_sched_barrier(0);
    if constexpr (s + PD < 28) ldstep(s + PD);
    __builtin_amdgcn_sched_barrier(0);
  });
  u4 xr0 = *(const u4*)(x + oidx), xr1 = *(const u4*)(x + oidx + 8);
  float* red = (float*)smem;
  auto put = [&](int a, int b) {
#pragma unroll
    for (int g = 0; g < 4; ++g) {
      f4 v = {acc[a][b][4 * g], acc[a][b][4 * g + 1], acc[a][b][4 * g + 2], acc[a][b][4 * g + 3]};
      *(f4*)(red + (w * 64 + b * 32 + r32) * 68 + a * 32 + 8 * g + 4 * hh) = v;
    }
  };
  STAMP(2);
#pragma unroll
  for (int a = 0; a < 2; ++a)
#pragma unroll
    for (int b = 0; b < 2; ++b)
      if (w != 2 * a + b) put(a, b);
  {
    BAR();
    STAMP(3);
    const int dt = w >> 1, tt = w & 1;
    const int yo = ((nt * 64 + dt * 32 + r32) * 2048 + mt * 64 + tt * 32 + 8 * hh) * 2;
    u4 y0, y1;
    for (unsigned spins = 0; spins < 4000000u; ++spins) {
      y0 = __builtin_amdgcn_raw_buffer_load_b128(yrs, yo, 0, 16);
      y1 = __builtin_amdgcn_raw_buffer_load_b128(yrs, yo + 32, 0, 16);
      bool bad = false;
#pragma unroll
      for (int p = 0; p < 4; ++p) bad = bad || y0[p] == 0xffffffffu || y1[p] == 0xffffffffu;
      if (__builtin_amdgcn_ballot_w64(bad) == 0ull) break;
      __builtin_amdgcn_s_sleep(2);
      asm volatile("" ::: "memory");
    }
    u4 i0, i1;
#pragma unroll
    for (int p = 0; p < 4; ++p) {
      int k = 8 * hh + 2 * p;
      i0[p] = (k == r32 ? 0x3f80u : 0u) | (k + 1 == r32 ? 0x3f800000u : 0u);
      i1[p] = (k + 16 == r32 ? 0x3f80u : 0u) | (k + 17 == r32 ? 0x3f800000u : 0u);
    }
    f16v t;
#pragma unroll
    for (int e = 0; e < 16; ++e) t[e] = 0.f;
    t = __builtin_amdgcn_mfma_f32_32x32x16_bf16(__builtin_bit_cast(bf8, y0), __builtin_bit_cast(bf8, i0), t, 0, 0, 0);
    t = __builtin_amdgcn_mfma_f32_32x32x16_bf16(__builtin_bit_cast(bf8, y1), __builtin_bit_cast(bf8, i1), t, 0, 0, 0);
#pragma unroll
    for (int a = 0; a < 2; ++a)
#pragma unroll
      for (int b = 0; b < 2; ++b)
        if (w == 2 * a + b) { acc[a][b] += t; put(a, b); }
  }
  STAMP(4); WAITLGKM(); BAR(); STAMP(5);
  u4 o[2];
#pragma unroll
  for (int q = 0; q < 4; ++q) {
    const float* rp = red + tok * 68 + dc + 4 * q;
    f4 v = (*(const f4*)rp + *(const f4*)(rp + 64 * 68)) + (*(const f4*)(rp + 128 * 68) + *(const f4*)(rp + 192 * 68));
    unsigned xa = q < 2 ? xr0[2 * q] : xr1[2 * q - 4], xb = q < 2 ? xr0[2 * q + 1] : xr1[2 * q - 3];
    o[q >> 1][2 * (q & 1)] = pk_bf(v[0] + bf_lo(xa), v[1] + bf_hi(xa));
    o[q >> 1][2 * (q & 1) + 1] = pk_bf(v[2] + bf_lo(xb), v[3] + bf_hi(xb));
  }
  *(u4*)(out + oidx) = o[0];
  *(u4*)(out + oidx + 8) = o[1];
  STAMP(6); STAMP_OUT(1);
}

extern "C" void kernel_launch(void* const* d_in, const int* in_sizes, int n_in, void* d_out, int out_size, void* d_ws,
                              size_t ws_size, hipStream_t stream) {
  (void)in_sizes; (void)n_in; (void)out_size; (void)ws_size;
  typedef const unsigned short* cp;
  (void)hipFuncSetAttribute((const void*)k1_kernel, hipFuncAttributeMaxDynamicSharedMemorySize, K1_LDS);
  (void)hipFuncSetAttribute((const void*)k2_kernel, hipFuncAttributeMaxDynamicSharedMemorySize, K2_LDS);
  k1_kernel<<<256, 256, K1_LDS, stream>>>((cp)d_in[0], (cp)d_in[8], (cp)d_in[9], (cp)d_in[10], (char*)d_ws, (cp)d_in[1], (cp)d_in[2],
                                          (cp)d_in[3], (cp)d_in[4], (cp)d_in[5], (cp)d_in[6], (cp)d_out);
  k2_kernel<<<256, 512, K2_LDS, stream>>>((cp)d_in[0], (cp)d_in[1], (cp)d_in[2], (cp)d_in[3], (cp)d_in[4], (cp)d_in[5],
                                          (cp)d_in[6], (char*)d_ws, (unsigned short*)d_out);
}
```

```cpp
#include <hip/hip_runtime.h>
#include <hip/hip_bf16.h>
#include <stdint.h>

typedef __attribute__((ext_vector_type(2))) __bf16 bf2;
typedef __attribute__((ext_vector_type(8))) __bf16 bf8;
typedef __attribute__((ext_vector_type(2))) _Float16 h2;
typedef __attribute__((ext_vector_type(8))) _Float16 h8;
typedef __attribute__((ext_vector_type(2))) float f2;
typedef __attribute__((ext_vector_type(4))) float f4;
typedef __attribute__((ext_vector_type(16))) float f16v;
typedef __attribute__((ext_vector_type(2))) unsigned u2;
typedef __attribute__((ext_vector_type(4))) unsigned u4;
typedef __attribute__((ext_vector_type(4))) short s4;
#define LDSP __attribute__((address_space(3)))
#define DEVI __device__ __forceinline__

constexpr int SEQ = 2048, DM = 512, FF = 1792;
constexpr int XT_ROW = 4608;
constexpr size_t OFF_H = 0;
constexpr size_t OFF_WF = OFF_H + (size_t)64 * 112 * 1024;
constexpr size_t OFF_XT = OFF_WF + (size_t)16 * 112 * 1024;
constexpr size_t OFF_YA = OFF_XT + (size_t)512 * XT_ROW + 1024;
constexpr size_t OFF_CNT = OFF_YA + (size_t)512 * 2048 * 2;

DEVI float bf_lo(unsigned v) { return __builtin_bit_cast(float, v << 16); }
DEVI float bf_hi(unsigned v) { return __builtin_bit_cast(float, v & 0xffff0000u); }
DEVI unsigned pk_bf(float a, float b) { f2 f = {a, b}; return __builtin_bit_cast(unsigned, __builtin_convertvector(f, bf2)); }
DEVI unsigned pk_h(float a, float b) { f2 f = {a, b}; return __builtin_bit_cast(unsigned, __builtin_convertvector(f, h2)); }
DEVI float rbf(float a) { return bf_lo(pk_bf(a, 0.f)); }
DEVI float ldbf(const unsigned short* p) { return __builtin_bit_cast(float, ((unsigned)*p) << 16); }
DEVI float dot2(unsigned a, unsigned b, float c) {
  return __builtin_amdgcn_fdot2_f32_bf16(__builtin_bit_cast(bf2, a), __builtin_bit_cast(bf2, b), c, false);
}
DEVI void glds16(const void* g, unsigned lds_base) {
  unsigned sv;
  asm volatile("s_mov_b32 %0, m0\n\ts_mov_b32 m0, %2\n\ts_nop 0\n\tglobal_load_lds_dwordx4 %1, off\n\ts_mov_b32 m0, %0"
               : "=&s"(sv) : "v"(g), "s"(lds_base) : "memory");
}
DEVI void glds16s(unsigned voff, const void* sbase, unsigned lds_base) {
  asm volatile("s_mov_b32 m0, %2\n\ts_nop 0\n\tglobal_load_lds_dwordx4 %0, %1" :: "v"(voff), "s"(sbase), "s"(lds_base) : "memory", "m0");
}
#define WAITVM(n) asm volatile("s_waitcnt vmcnt(" #n ")" ::: "memory")
#define WAITLGKM() asm volatile("s_waitcnt lgkmcnt(0)" ::: "memory")
#define BAR() do { asm volatile("" ::: "memory"); __builtin_amdgcn_s_barrier(); asm volatile("" ::: "memory"); } while (0)
#define GLD(dst, voff, sptr, imm) asm volatile("global_load_dwordx4 %0, %1, %2 offset:" #imm : "=v"(dst) : "v"(voff), "s"(sptr))
#define GLDH(dst, voff, sptr) asm volatile("global_load_ushort %0, %1, %2" : "=v"(dst) : "v"(voff), "s"(sptr))
#define SB() __builtin_amdgcn_sched_barrier(0)
template <int V> struct ic { static constexpr int value = V; };
template <int I, int N, class F> DEVI void static_for(F&& f) { if constexpr (I < N) { f(ic<I>{}); static_for<I + 1, N>(f); } }
template <int N> DEVI void waitvm() { asm volatile("s_waitcnt vmcnt(%0)" ::"n"(N) : "memory"); }

#define STAMP(i)
#define STAMP_DECL
#define STAMP_OUT(k)
struct cf { float r, i; };
DEVI cf cmul(cf a, cf b) { cf c; c.r = a.r * b.r - a.i * b.i; c.i = a.r * b.i + a.i * b.r; return c; }
DEVI cf zpow(float a2, float rev, float t) {
  float e = __builtin_amdgcn_exp2f(a2 * t);
  float p = __builtin_amdgcn_fractf(rev * t);
  cf c; c.r = e * __builtin_amdgcn_cosf(p); c.i = e * __builtin_amdgcn_sinf(p); return c;
}
DEVI float wave_sum(float v) {
#pragma unroll
  for (int o = 32; o >= 1; o >>= 1) v += __shfl_xor(v, o);
  return v;
}

constexpr int A_S = 16, A_W = 16, A_H = 16, A_O = 0;
constexpr int K1_LDS = 10 * 16384;

DEVI void kgen_wave(char* sa, char* sb, int lane, float a2, float rev, cf C) {
  char* wq = sa + lane * 4; char* w0 = wq + 4352; char* w1 = sb + lane * 4; char* w2 = w1 + 4352;
  float* tab = (float*)(sa + 64);
  const int rd = (lane & 15) * 272 + (lane >> 4) * 16;
  cf z1 = zpow(a2, rev, 1.f), z16 = zpow(a2, rev, 16.f), z256 = zpow(a2, rev, 256.f), z1024 = zpow(a2, rev, 1024.f);
  const cf z32 = cmul(z16, z16), z2 = cmul(z1, z1), z512 = cmul(z256, z256), z768 = cmul(z512, z256);
  cf wz[4]; wz[0] = z1024; wz[1] = cmul(z1024, z256); wz[2] = cmul(z1024, z512); wz[3] = cmul(z1024, z768);
  cf e = {1.f, 0.f}, o = z16, pe = C, po = cmul(C, z1);
  { cf p = cmul(cmul(C, z1024), z1024); *(unsigned*)(w2 + 8 * 272) = pk_h(p.r, p.i); }
#pragma unroll
  for (int r = 0; r < 8; ++r) {
    *(unsigned*)(wq + (2 * r) * 272) = pk_h(e.r, -e.i);
    *(unsigned*)(wq + (2 * r + 1) * 272) = pk_h(o.r, -o.i);
    *(unsigned*)(w0 + (2 * r) * 272) = pk_h(pe.r, pe.i);
    *(unsigned*)(w0 + (2 * r + 1) * 272) = pk_h(po.r, po.i);
    { cf p = cmul(pe, z256); *(unsigned*)(w1 + r * 272) = pk_h(p.r, p.i); }
    if ((r & 1) == 0) {
      cf p = cmul(pe, z512), q = cmul(pe, z768);
      *(unsigned*)(w1 + (8 + (r >> 1)) * 272) = pk_h(p.r, p.i); *(unsigned*)(w1 + (12 + (r >> 1)) * 272) = pk_h(q.r, q.i);
    }
    if ((r & 3) == 0) {
#pragma unroll
      for (int f = 0; f < 4; ++f) { cf p = cmul(pe, wz[f]); *(unsigned*)(w2 + (2 * f + (r >> 2)) * 272) = pk_h(p.r, p.i); }
    }
    if (r < 7) { e = cmul(e, z32); o = cmul(o, z32); pe = cmul(pe, z2); po = cmul(po, z2); }
  }
  f4 a0 = {0.f, 0.f, 0.f, 0.f}, a1 = a0, a2v = a0;
#pragma unroll
  for (int s = 0; s < 4; ++s) {
    const h8 qf = *(const h8*)(sa + rd + s * 64);
    a0 = __builtin_amdgcn_mfma_f32_16x16x32_f16(*(const h8*)(sa + 4352 + rd + s * 64), qf, a0, 0, 0, 0);
    a1 = __builtin_amdgcn_mfma_f32_16x16x32_f16(*(const h8*)(sb + rd + s * 64), qf, a1, 0, 0, 0);
    a2v = __builtin_amdgcn_mfma_f32_16x16x32_f16(*(const h8*)(sb + 4352 + rd + s * 64), qf, a2v, 0, 0, 0);
  }
  const int col = lane & 15, rq = lane >> 4;
  *(f4*)(tab + 4 * rq + 16 * col) = a0;
  *(f4*)(tab + ((rq < 2) ? (256 + 4 * rq + 8 * col) : (384 + 64 * (rq & 1) + 4 * col))) = a1;
  if (rq < 2) {
    *(f2*)(tab + 512 + 64 * rq + 2 * col) = f2{a2v[0], a2v[1]};
    *(f2*)(tab + 512 + 64 * rq + 32 + 2 * col) = f2{a2v[2], a2v[3]};
  } else if (lane == 32) {
    tab[640] = a2v[0];
  }
}

DEVI void kput(char* kc, int blk, const float* v) {
  unsigned E[5], O[5];
#pragma unroll
  for (int k = 0; k < 5; ++k) { E[k] = pk_h(v[2 * k], v[2 * k + 1]); O[k] = pk_h(v[2 * k + 1], v[2 * k + 2]); }
  *(u4*)(kc + 16 * blk) = u4{E[0], E[1], E[2], E[3]};
  *(u4*)(kc + 4160 + 16 * blk) = u4{O[0], O[1], O[2], O[3]};
  *(u4*)(kc + 2 * 4160 + 16 * blk) = u4{E[1], E[2], E[3], E[4]};
  *(u4*)(kc + 3 * 4160 + 16 * blk) = u4{O[1], O[2], O[3], O[4]};
}
DEVI void kexpand(const float* tA, const float* tB, int t, int par, char* kc) {
  float v[11];
  {
    const int m = 255 - t;
    float z = tA[383 + m] + tB[383 + m], a = tA[384 + m] + tB[384 + m], b = tA[385 + m] + tB[385 + m];
    v[0] = b; v[1] = b; v[2] = b; v[3] = (m & 1) ? b : a; v[4] = a; v[5] = a; v[6] = a; v[7] = a; v[8] = a; v[9] = a;
    v[10] = m == 128 ? z : a;
    kput(kc, t, v);
  }
  const int m = 127 - t;
  if (par == 0) {
    const int bs = 256 + 2 * m;
    float y = tA[bs - 2] + tB[bs - 2], z = tA[bs - 1] + tB[bs - 1], a = tA[bs] + tB[bs], b = tA[bs + 1] + tB[bs + 1], c = tA[bs + 2] + tB[bs + 2];
    v[0] = c; v[1] = c; v[2] = b; v[3] = b; v[4] = b; v[5] = a; v[6] = a; v[7] = a; v[8] = a;
    v[9] = m == 64 ? z : a; v[10] = m == 64 ? y : z;
  } else {
    const bool s0 = m < 32, s1 = m <= 32, nz = m != 0;
    const int bs = s0 ? 8 * m - 3 : 125 + 4 * m;
    float W[11];
#pragma unroll
    for (int i = 0; i < 11; ++i) W[i] = tA[bs + i] + tB[bs + i];
    v[0] = s0 ? W[10] : W[7]; v[1] = s0 ? W[9] : W[6]; v[2] = s0 ? W[8] : W[5]; v[3] = s0 ? W[7] : W[5];
    v[4] = s0 ? W[6] : W[5]; v[5] = s0 ? W[5] : W[4]; v[6] = s0 ? W[4] : W[3]; v[7] = W[3];
    v[8] = nz ? (s1 ? W[2] : W[3]) : 0.f; v[9] = nz ? (s1 ? W[1] : W[2]) : 0.f; v[10] = nz ? (s1 ? W[0] : W[1]) : 0.f;
  }
  kput(kc, 128 + t, v);
}

DEVI bf8 tr_frag(unsigned addr) {
  s4 a = __builtin_amdgcn_ds_read_tr16_b64_v4i16((LDSP s4*)(uintptr_t)addr);
  s4 b = __builtin_amdgcn_ds_read_tr16_b64_v4i16((LDSP s4*)(uintptr_t)(addr + 256));
  typedef __attribute__((ext_vector_type(8))) short s8;
  s8 r = __builtin_shufflevector(a, b, 0, 1, 2, 3, 4, 5, 6, 7);
  return __builtin_bit_cast(bf8, r);
}
DEVI float ln_inv(float var) { return 1.f / rbf(__builtin_sqrtf(rbf(rbf(fmaxf(var, 0.f)) + 1e-5f))); }

DEVI void warm(const void* a0, const void* a1, const void* a2, const void* a3, const void* b0, const void* b1, const void* b2,
               unsigned lds0) {
  const unsigned t = threadIdx.x, bid = blockIdx.x;
  const unsigned w = __builtin_amdgcn_readfirstlane(t >> 6);
  const uintptr_t x0 = (w & 1) ? (uintptr_t)a1 : (uintptr_t)a0, x1 = (w & 1) ? (uintptr_t)a3 : (uintptr_t)a2;
  const uintptr_t y0 = (w & 1) ? (uintptr_t)b1 : (uintptr_t)b0;
  const uintptr_t pa = ((w & 2) ? x1 : x0) + bid * 512u;
  const uintptr_t pb = (w & 2) ? (uintptr_t)b2 + (bid * 8192u + (w & 1) * 4096u) : y0 + ((bid * 4u) & ~15u);
  const uintptr_t q = (t & 32) ? pb : pa + (t & 31) * 16u;
  glds16((const void*)q, lds0 + 5 * 16384 + w * 1024);
}

constexpr int slot_of(int s) { return (s + 8) % 10; }

__global__ void __launch_bounds__(256, 1)
k1_kernel(const unsigned short* __restrict__ x, const unsigned short* __restrict__ wg, const unsigned short* __restrict__ wu,
          const unsigned short* __restrict__ wd, char* __restrict__ ws, const unsigned short* __restrict__ logA,
          const unsigned short* __restrict__ Aim, const unsigned short* __restrict__ Cre, const unsigned short* __restrict__ Cim,
          const unsigned short* __restrict__ logdt, const unsigned short* __restrict__ dskip, const unsigned short* out) {
  extern __shared__ __attribute__((aligned(16))) char smem[];
  const int tid = threadIdx.x, lane = tid & 63;
  const int w = __builtin_amdgcn_readfirstlane(tid >> 6);
  const int bid = blockIdx.x, xcd = bid & 7, idx = bid >> 3;
  const int mt = 4 * (xcd >> 1) + idx / 7, ft = 7 * (xcd & 1) + idx % 7;
  const unsigned lds0 = (unsigned)(uintptr_t)(LDSP char*)smem;
  const int r32 = lane & 31, hh = lane >> 5;
  auto wrs = __builtin_amdgcn_make_buffer_rsrc((void*)ws, (short)0, (int)OFF_CNT, 0x00020000);
  STAMP_DECL; STAMP(0);
  {
    const u4 m = {~0u, ~0u, ~0u, ~0u};
    __builtin_amdgcn_raw_buffer_store_b128(m, wrs, (unsigned)OFF_YA + bid * 8192 + tid * 16, 0, A_S);
    __builtin_amdgcn_raw_buffer_store_b128(m, wrs, (unsigned)OFF_YA + bid * 8192 + 4096 + tid * 16, 0, A_S);
  }

  if (idx >= 28) {
    const int sp = xcd * 4 + (idx - 28);
    {
      const unsigned short* xp = x + (size_t)(sp * 64 + lane) * DM + w * 128;
      u4 xv[16];
#pragma unroll
      for (int j = 0; j < 16; ++j) xv[j] = *(const u4*)(xp + 8 * j);
      float s0 = 0.f, s1 = 0.f, q0 = 0.f, q1 = 0.f;
#pragma unroll
      for (int j = 0; j < 16; ++j)
#pragma unroll
        for (int p = 0; p < 4; p += 2) {
          s0 = dot2(xv[j][p], 0x3f803f80u, s0); s1 = dot2(xv[j][p + 1], 0x3f803f80u, s1);
          q0 = dot2(xv[j][p], xv[j][p], q0); q1 = dot2(xv[j][p + 1], xv[j][p + 1], q1);
        }
      f2* st2 = (f2*)smem;
      st2[w * 64 + lane] = f2{s0 + s1, q0 + q1};
      STAMP(3);
      __syncthreads();
      const f2 tt = (st2[lane] + st2[64 + lane]) + (st2[128 + lane] + st2[192 + lane]);
      const float mean = tt[0] * (1.f / 512.f), mb = rbf(mean);
      const float inv = ln_inv(tt[1] * (1.f / 512.f) - mean * mean);
      char* xd = ws + OFF_XT + (size_t)(w * 128) * XT_ROW + 144 * sp;
      const unsigned xo = (lane & 1) * XT_ROW + (lane >> 1) * 4, sel = (lane & 1) ? 0x03020706u : 0x05040100u;
#pragma unroll
      for (int j = 0; j < 16; ++j)
#pragma unroll
        for (int p = 0; p < 4; ++p) {
          unsigned d = pk_bf(bf_lo(xv[j][p]) - mb, bf_hi(xv[j][p]) - mb);
          unsigned n = pk_bf(bf_lo(d) * inv, bf_hi(d) * inv);
          unsigned hv = pk_h(bf_lo(n), bf_hi(n));
          unsigned ot = __builtin_amdgcn_update_dpp(0u, hv, 0xB1, 0xf, 0xf, false);
          *(unsigned*)(xd + (size_t)(8 * j + 2 * p) * XT_ROW + xo) = __builtin_amdgcn_perm(ot, hv, sel);
        }
    }
    STAMP(1);
    warm(logA, Aim, Cre, Cim, logdt, dskip, out, lds0);
    const int fr0 = (sp * 4 + w) * 14;
#pragma unroll 1
    for (int b = 0; b < 2; ++b) {
      unsigned short v[7][8];
      const int fr = fr0 + b * 7, nt = fr / 112, ks = fr % 112;
      const unsigned short* p = wd + (size_t)(16 * ks + 4 * hh) * DM + 32 * nt + r32;
#pragma unroll
      for (int i = 0; i < 7; ++i)
#pragma unroll
        for (int j = 0; j < 8; ++j) v[i][j] = p[(16 * i + 8 * (j >> 2) + (j & 3)) * DM];
#pragma unroll
      for (int i = 0; i < 7; ++i) {
        u4 o;
#pragma unroll
        for (int j = 0; j < 4; ++j) o[j] = (unsigned)v[i][2 * j] | ((unsigned)v[i][2 * j + 1] << 16);
        __builtin_amdgcn_raw_buffer_store_b128(o, wrs, (unsigned)OFF_WF + (fr + i) * 1024 + lane * 16, 0, A_W);
      }
    }
    STAMP(2); STAMP_OUT(0);
    WAITVM(0);
    return;
  }

  const unsigned wvo = ((8 * w + ((lane >> 2) & 7)) * FF + ft * 128 + (lane >> 5) * 32 + (lane & 3) * 8) * 2;
  const unsigned xsl = lds0 + w * 4096;
  auto issue_piece = [&](int s, int q) {
    glds16s(wvo, (q & 2 ? wu : wg) + (size_t)s * 32 * FF + (q & 1) * 64, xsl + slot_of(s) * 16384 + q * 1024);
  };
  auto issue_slab = [&](int s) { issue_piece(s, 0); issue_piece(s, 1); issue_piece(s, 2); issue_piece(s, 3); };
  u4 xf[32];
  float mean, var;
  {
    unsigned xvo[4];
#pragma unroll
    for (int j = 0; j < 4; ++j) xvo[j] = (mt * 128 + w * 32 + 4 * j + (lane >> 4)) * 1024 + ((((lane & 15) ^ (lane >> 4)) ^ (4 * j)) * 16);
    auto issue_x = [&](int c) {
#pragma unroll
      for (int j = 0; j < 8; ++j) glds16s(xvo[j & 3], x + 128 * c + (j >> 2) * 8192, xsl + c * 32768 + (j >> 2) * 16384 + 1024 * (j & 3));
    };
    issue_x(0); issue_x(1); issue_x(2);
    unsigned xb0 = xsl + (r32 >> 4) * 16384 + (r32 & 15) * 256, xb1 = xb0 + 65536;
    asm volatile("" : "+v"(xb0), "+v"(xb1));
    float s0 = 0.f, s1 = 0.f, q0 = 0.f, q1 = 0.f;
    static_for<0, 4>([&](auto H) {
      constexpr int c = H.value;
      waitvm<16>();
#pragma unroll
      for (int s = 0; s < 8; ++s)
        xf[8 * c + s] = *(const LDSP u4*)(uintptr_t)(((c & 2) ? xb1 : xb0) + (((2 * s + hh) ^ (r32 & 15)) * 16) + (c & 1) * 32768);
      if constexpr (c == 0) issue_x(3);
      if constexpr (c == 1) { issue_slab(0); issue_slab(1); }
#pragma unroll
      for (int s = 8 * c; s < 8 * c + 8; ++s)
#pragma unroll
        for (int p = 0; p < 4; p += 2) {
          s0 = dot2(xf[s][p], 0x3f803f80u, s0); s1 = dot2(xf[s][p + 1], 0x3f803f80u, s1);
          q0 = dot2(xf[s][p], xf[s][p], q0); q1 = dot2(xf[s][p + 1], xf[s][p + 1], q1);
        }
      asm volatile("" ::"v"(s0), "v"(s1), "v"(q0), "v"(q1) : "memory");
      if constexpr (c == 2) { issue_slab(2); issue_slab(3); }
      if constexpr (c == 3) issue_slab(4);
    });
    STAMP(1);
    float t = s0 + s1, q = q0 + q1;
    t += __shfl_xor(t, 32); q += __shfl_xor(q, 32);
    mean = t * (1.f / 512.f);
    var = q * (1.f / 512.f) - mean * mean;
  }
  STAMP(2);
  const float mb = rbf(mean);
  auto center = [&](int s, int p) { xf[s][p] = pk_bf(bf_lo(xf[s][p]) - mb, bf_hi(xf[s][p]) - mb); };
#pragma unroll
  for (int p = 0; p < 8; ++p) center(p >> 2, p & 3);
  f16v acc[8];
#pragma unroll
  for (int c = 0; c < 8; ++c)
#pragma unroll
    for (int e = 0; e < 16; ++e) acc[c][e] = 0.f;
  unsigned tb[3];
  tb[0] = lds0 + hh * 4096 + ((lane & 15) >> 2) * 64 + ((lane >> 4) & 1) * 32 + (lane & 3) * 8;
  tb[1] = tb[0] + 65536; tb[2] = tb[0] + 131072;
  asm volatile("" : "+v"(tb[0]), "+v"(tb[1]), "+v"(tb[2]));
  auto wfrag = [&](int n, int c) {
    const int sl = slot_of(n >> 2), nG = n & 3;
    return tr_frag(tb[sl >> 2] + (sl & 3) * 16384 + (nG >> 1) * 8192 + (nG & 1) * 2048 + c * 512);
  };
  waitvm<16>();
  BAR();
  bf8 wf[3][4];
#pragma unroll
  for (int c = 0; c < 8; ++c) wf[c >> 2][c & 3] = wfrag(c >> 2, c & 3);
  STAMP(4);
  SB();
  static_for<0, 64>([&](auto GG) {
    constexpr int g = GG.value, i = g >> 2, G = g & 3;
    if constexpr (G == 1 && i < 15) {
      waitvm<(i <= 11 ? 12 : 4 * (14 - i))>();
      BAR();
    }
    __builtin_amdgcn_s_waitcnt(g < 63 ? 0xC87F : 0xC07F);
    SB();
    const bf8 xb = __builtin_bit_cast(bf8, xf[2 * i + (G >> 1)]);
#pragma unroll
    for (int c = 0; c < 4; ++c) {
      acc[(G & 1) * 4 + c] = __builtin_amdgcn_mfma_f32_32x32x16_bf16(wf[g % 3][c], xb, acc[(G & 1) * 4 + c], 0, 0, 0);
      if constexpr (g + 2 < 64) wf[(g + 2) % 3][c] = wfrag(g + 2, c);
      if constexpr (g >= 1 && (g - 1) / 4 + 5 <= 15) { if (c == 1) issue_piece((g - 1) / 4 + 5, (g - 1) & 3); }
      if constexpr (i < 15) { if ((c & 1) == 0) center(2 * i + 2 + (G >> 1), (G & 1) * 2 + (c >> 1)); }
      SB();
    }
  });

  STAMP(5);
  warm(logA, Aim, Cre, Cim, logdt, dskip, out, lds0);
  const float inv = ln_inv(var);
  const float c1 = -inv * 1.44269504088896f, c2 = inv * inv;
  const unsigned hdst = (unsigned)OFF_H + ((mt * 4 + w) * 112 + ft * 8) * 1024 + lane * 16;
#pragma unroll
  for (int c = 0; c < 4; ++c) {
    unsigned o[8];
#pragma unroll
    for (int p = 0; p < 8; ++p) {
      f2 g = f2{acc[c][2 * p], acc[c][2 * p + 1]};
      f2 u = f2{acc[4 + c][2 * p], acc[4 + c][2 * p + 1]};
      f2 t = g * c1;
      f2 ex = f2{__builtin_amdgcn_exp2f(t[0]), __builtin_amdgcn_exp2f(t[1])} + 1.f;
      f2 r = f2{__builtin_amdgcn_rcpf(ex[0]), __builtin_amdgcn_rcpf(ex[1])};
      f2 hv = (g * u) * (r * c2);
      o[p] = pk_bf(hv[0], hv[1]);
    }
    u4 a = {o[0], o[1], o[2], o[3]}, b = {o[4], o[5], o[6], o[7]};
    __builtin_amdgcn_raw_buffer_store_b128(a, wrs, hdst + (2 * c) * 1024, 0, A_H);
    __builtin_amdgcn_raw_buffer_store_b128(b, wrs, hdst + (2 * c + 1) * 1024, 0, A_H);
    SB();
  }
  STAMP(6); STAMP_OUT(0);
  WAITVM(0);
}

constexpr int K2_RED = 4 * 64 * 272;
constexpr int C_XS = 0, C_K = 7424, C_Y = 7424 + 4 * 4160, C_SZ = C_Y + 20480;
constexpr int K2_SYNC = K2_RED + 2 * C_SZ;
constexpr int K2_LDS = K2_SYNC + 64;

DEVI void pair_sync(LDSP unsigned* c, unsigned target, int lane) {
  asm volatile("s_waitcnt vmcnt(0) lgkmcnt(0)" ::: "memory");
  if (lane == 0) __hip_atomic_fetch_add(c, 1u, __ATOMIC_RELAXED, __HIP_MEMORY_SCOPE_WORKGROUP);
  while (__hip_atomic_load(c, __ATOMIC_RELAXED, __HIP_MEMORY_SCOPE_WORKGROUP) < target) __builtin_amdgcn_s_sleep(1);
  asm volatile("" ::: "memory");
}
DEVI h8 kfrag(const LDSP char* p, const LDSP char* p8, int off) {
  u2 a = *(const LDSP u2*)(p + off), b = *(const LDSP u2*)(p8 + off);
  u4 r = {a[0], a[1], b[0], b[1]};
  return __builtin_bit_cast(h8, r);
}
constexpr int xoff(int v) { return 32 * v + 16 * (v >> 2); }

template <int U0, int U1>
DEVI void conv_near(const LDSP char* xa, const LDSP char* kb, const LDSP char* kb8, f16v& a0, f16v& a1) {
  constexpr int D = 3;
  h8 xq[D + 2], kq[D];
#pragma unroll
  for (int i = 0; i < D + 2; ++i) xq[i] = *(const LDSP h8*)(xa + xoff(U0 + i - 64));
#pragma unroll
  for (int i = 0; i < D; ++i) kq[i] = kfrag(kb, kb8, 32 * (U0 + i));
  SB();
  static_for<U0, U1>([&](auto U) {
    constexpr int u = U.value, i = u - U0;
    a0 = __builtin_amdgcn_mfma_f32_32x32x16_f16(xq[i % (D + 2)], kq[i % D], a0, 0, 0, 0);
    if constexpr (u + D < U1) xq[i % (D + 2)] = *(const LDSP h8*)(xa + xoff(u + D + 2 - 64));
    SB();
    a1 = __builtin_amdgcn_mfma_f32_32x32x16_f16(xq[(i + 2) % (D + 2)], kq[i % D], a1, 0, 0, 0);
    if constexpr (u + D < U1) kq[i % D] = kfrag(kb, kb8, 32 * (u + D));
    SB();
  });
}

__global__ void __launch_bounds__(512, 2)
k2_kernel(const unsigned short* __restrict__ x, const unsigned short* __restrict__ logA, const unsigned short* __restrict__ Aim,
          const unsigned short* __restrict__ Cre, const unsigned short* __restrict__ Cim, const unsigned short* __restrict__ logdt,
          const unsigned short* __restrict__ dskip, char* __restrict__ ws,
          unsigned short* __restrict__ out) {
  extern __shared__ __attribute__((aligned(16))) char smem[];
  const int tid = threadIdx.x, lane = tid & 63;
  const int w = __builtin_amdgcn_readfirstlane(tid >> 6);
  const int bid = blockIdx.x, xcd = bid & 7, idx = bid >> 3;
  const unsigned lds0 = (unsigned)(uintptr_t)(LDSP char*)smem;
  const int r32 = lane & 31, hh = lane >> 5;
  LDSP unsigned* sy = (LDSP unsigned*)(uintptr_t)(lds0 + K2_SYNC);
  auto yrs = __builtin_amdgcn_make_buffer_rsrc((void*)(ws + OFF_YA), (short)0, 512 * 2048 * 2, 0x00020000);

  STAMP_DECL; STAMP(0);
  unsigned pla_ = 0, pai_ = 0, pcr_ = 0, pci_ = 0, qcr_ = 0, qci_ = 0, pld_ = 0, dsk_ = 0;
  if (w >= 4) {
    __builtin_amdgcn_s_setprio(3);
    const unsigned nc = (2 * bid + ((w - 4) >> 1)) * 2, n0 = nc * 128 + ((w & 1) * 64 + lane) * 2, n1 = n0 ^ 128u;
    GLDH(pla_, n0, logA); GLDH(pai_, n0, Aim); GLDH(pcr_, n0, Cre); GLDH(pci_, n0, Cim);
    GLDH(qcr_, n1, Cre); GLDH(qci_, n1, Cim); GLDH(pld_, nc, logdt); GLDH(dsk_, nc, dskip);
  }
  if (w == 4 && lane < 16) sy[lane] = 0u;
  WAITLGKM(); BAR();
  if (w >= 4) {
    const int cl = (w - 4) >> 1, wv = w & 1, ch = 2 * bid + cl;
    char* cb = smem + K2_RED + cl * C_SZ;
    const unsigned cl0 = lds0 + K2_RED + cl * C_SZ;
    {
      const char* xsrc = ws + OFF_XT + (size_t)ch * XT_ROW + lane * 16;
      if (wv == 0) { glds16(xsrc, cl0 + 2304); glds16(xsrc + 1024, cl0 + 3328); glds16(xsrc + 2048, cl0 + 4352); }
      else { glds16(xsrc + 3072, cl0 + 5376); glds16(xsrc + 4096, cl0 + 6400); glds16(xsrc + 2048, cl0 + 4352); }
      u4 z = {0u, 0u, 0u, 0u};
      if (wv == 0) {
        *(u4*)(cb + lane * 16) = z; *(u4*)(cb + 1024 + lane * 16) = z;
        if (lane < 16) *(u4*)(cb + 2048 + lane * 16) = z;
      }
    }
    asm volatile("s_waitcnt vmcnt(3)" : "+v"(pla_), "+v"(pai_), "+v"(pcr_), "+v"(pci_), "+v"(qcr_), "+v"(qci_), "+v"(pld_), "+v"(dsk_));
    const float pla = bf_lo(pla_), pai = bf_lo(pai_), pcr = bf_lo(pcr_), pci = bf_lo(pci_), qcr = bf_lo(qcr_), qci = bf_lo(qci_);
    const float pld = bf_lo(pld_), dsk = bf_lo(dsk_);
    float scale;
    {
      float dt = rbf(__expf(pld));
      float are = -rbf(__expf(pla));
      float a2 = (are * dt) * 1.44269504088896f, rev = (pai * dt) * 0.159154943091895f;
      scale = 2.f * dt * wave_sum(fabsf(pcr) + fabsf(pci) + fabsf(qcr) + fabsf(qci));
      float isc = scale > 0.f ? 2.f * dt / scale : 0.f;
      cf C = {pcr * isc, pci * isc};
      STAMP(5);
      kgen_wave(cb + C_Y + wv * 10240, cb + C_K + wv * 8320, lane, a2, rev, C);
      STAMP(6);
      pair_sync(sy + 6 + cl, 2u, lane);
      const float* tA = (const float*)(cb + C_Y + 64);
      kexpand(tA, tA + 2560, wv * 64 + lane, wv, cb + C_K);
      if (wv == 0 && lane < 16) *(u4*)(cb + C_K + (lane >> 2) * 4160 + 4096 + (lane & 3) * 16) = u4{0u, 0u, 0u, 0u};
    }
    pair_sync(sy + cl, 2u, lane);
    STAMP(1);
    const int cc = (3 - r32) & 3;
    const LDSP char* cbl = (const LDSP char*)(uintptr_t)cl0;
    const LDSP char* kb = cbl + C_K + cc * 4160 + 2 * (8 * hh - r32 - 1 - cc);
    const LDSP char* kb8 = kb + 8;
    asm volatile("" : "+v"(kb8));
    const LDSP char* xa = cbl + 144 * r32 + 16 * hh;
    float* yb = (float*)(cb + C_Y);
    f16v a0, a1;
#pragma unroll
    for (int e = 0; e < 16; ++e) { a0[e] = 0.f; a1[e] = 0.f; }
    if (wv == 0) {
      conv_near<82, 130>(xa, kb, kb8, a0, a1);
    } else {
      const LDSP char* xfa = cbl + 144 * (r32 >> 1) + 16 * hh + 64 * (r32 & 1);
      const LDSP char* xfb = xfa + 16 * (r32 & 1);
      constexpr int D = 4;
      h8 xq[D], kq[D];
#pragma unroll
      for (int i = 0; i < D; ++i) { xq[i] = *(const LDSP h8*)((((2 + i) & 2) ? xfb : xfa) + xoff(2 + i)); kq[i] = kfrag(kb, kb8, 32 * (2 + i)); }
      SB();
      static_for<2, 66>([&](auto U) {
        constexpr int u = U.value, i = (u - 2) % D;
        if constexpr (u & 1) a1 = __builtin_amdgcn_mfma_f32_32x32x16_f16(xq[i], kq[i], a1, 0, 0, 0);
        else a0 = __builtin_amdgcn_mfma_f32_32x32x16_f16(xq[i], kq[i], a0, 0, 0, 0);
        if constexpr (u + D < 66) { xq[i] = *(const LDSP h8*)((((u + D) & 2) ? xfb : xfa) + xoff(u + D)); kq[i] = kfrag(kb, kb8, 32 * (u + D)); }
        SB();
      });
#pragma unroll
      for (int e = 0; e < 16; ++e) { yb[4096 + 32 * ((e & 3) + 8 * (e >> 2) + 4 * hh) + r32] = a0[e] + a1[e]; a0[e] = 0.f; a1[e] = 0.f; }
      conv_near<66, 82>(xa, kb, kb8, a0, a1);
    }
#pragma unroll
    for (int e = 0; e < 16; ++e) {
      int a = (e & 3) + 8 * (e >> 2) + 4 * hh;
      yb[2048 * wv + 64 * a + r32] = a0[e];
      yb[2048 * wv + 64 * a + 32 + r32] = a1[e];
    }
    STAMP(2);
    pair_sync(sy + 2 + cl, 2u, lane);
    STAMP(3);
#pragma unroll
    for (int rnd = 0; rnd < 2; ++rnd) {
      const int l = 1024 * wv + 512 * rnd + 8 * lane;
      f4 p0 = *(const f4*)(yb + l), p1 = *(const f4*)(yb + l + 4);
      p0 += *(const f4*)(yb + 2048 + l); p1 += *(const f4*)(yb + 2048 + l + 4);
      if (wv) { p0 += *(const f4*)(yb + 3072 + l); p1 += *(const f4*)(yb + 3072 + l + 4); }
      h8 xv = *(const h8*)(cb + 2304 + 2 * l + 16 * (l >> 6));
      u4 o;
#pragma unroll
      for (int p = 0; p < 4; ++p) {
        float y0 = p < 2 ? p0[2 * p] : p1[2 * p - 4], y1 = p < 2 ? p0[2 * p + 1] : p1[2 * p - 3];
        unsigned yv = pk_bf(y0 * scale, y1 * scale);
        unsigned sv = pk_bf((float)xv[2 * p] * dsk, (float)xv[2 * p + 1] * dsk);
        o[p] = pk_bf(bf_lo(yv) + bf_lo(sv), bf_hi(yv) + bf_hi(sv));
        o[p] = o[p] == 0xffffffffu ? 0xfffeffffu : o[p];
      }
      __builtin_amdgcn_raw_buffer_store_b128(o, yrs, (ch * 2048 + l) * 2, 0, 16);
    }
    STAMP(4); STAMP_OUT(1);
    return;
  }

  const int mt = 4 * xcd + (idx >> 3), nt = idx & 7;
  const char* hp = ws + OFF_H + ((size_t)(2 * mt) * 112 + 28 * w) * 1024;
  const char* wp = ws + OFF_WF + ((size_t)(2 * nt) * 112 + 28 * w) * 1024;
  const unsigned vo = lane * 16;
  constexpr int PD = 7;
  u4 hb[PD][2], wb[PD][2];
  auto ldstep = [&](int s) {
    const int b = s % PD, hi = (s >> 2) * 4096;
    switch (s & 3) {
      case 0: GLD(hb[b][0], vo, hp + hi, 0); GLD(wb[b][0], vo, wp + hi, 0); GLD(hb[b][1], vo, hp + 112 * 1024 + hi, 0); GLD(wb[b][1], vo, wp + 112 * 1024 + hi, 0); break;
      case 1: GLD(hb[b][0], vo, hp + hi, 1024); GLD(wb[b][0], vo, wp + hi, 1024); GLD(hb[b][1], vo, hp + 112 * 1024 + hi, 1024); GLD(wb[b][1], vo, wp + 112 * 1024 + hi, 1024); break;
      case 2: GLD(hb[b][0], vo, hp + hi, 2048); GLD(wb[b][0], vo, wp + hi, 2048); GLD(hb[b][1], vo, hp + 112 * 1024 + hi, 2048); GLD(wb[b][1], vo, wp + 112 * 1024 + hi, 2048); break;
      default: GLD(hb[b][0], vo, hp + hi, 3072); GLD(wb[b][0], vo, wp + hi, 3072); GLD(hb[b][1], vo, hp + 112 * 1024 + hi, 3072); GLD(wb[b][1], vo, wp + 112 * 1024 + hi, 3072); break;
    }
  };
  __builtin_amdgcn_s_sleep(8);
#pragma unroll
  for (int s = 0; s < PD; ++s) ldstep(s);
  const int tok = tid >> 2, dc = (tid & 3) * 16;
  const size_t oidx = (size_t)(mt * 64 + tok) * DM + nt * 64 + dc;
  STAMP(1);
  f16v acc[2][2];
#pragma unroll
  for (int e = 0; e < 16; ++e) { acc[0][0][e] = 0.f; acc[0][1][e] = 0.f; acc[1][0][e] = 0.f; acc[1][1][e] = 0.f; }
  static_for<0, 28>([&](auto S) {
    constexpr int s = S.value, b = s % PD;
    constexpr int left = (28 - 1 - s) < (PD - 1) ? (28 - 1 - s) : (PD - 1);
    asm volatile("s_waitcnt vmcnt(%4)" : "+v"(hb[b][0]), "+v"(hb[b][1]), "+v"(wb[b][0]), "+v"(wb[b][1]) : "n"(4 * left));
    const bf8 h0 = __builtin_bit_cast(bf8, hb[b][0]), h1 = __builtin_bit_cast(bf8, hb[b][1]);
    const bf8 w0 = __builtin_bit_cast(bf8, wb[b][0]), w1 = __builtin_bit_cast(bf8, wb[b][1]);
    acc[0][0] = __builtin_amdgcn_mfma_f32_32x32x16_bf16(w0, h0, acc[0][0], 0, 0, 0);
    acc[0][1] = __builtin_amdgcn_mfma_f32_32x32x16_bf16(w0, h1, acc[0][1], 0, 0, 0);
    acc[1][0] = __builtin_amdgcn_mfma_f32_32x32x16_bf16(w1, h0, acc[1][0], 0, 0, 0);
    acc[1][1] = __builtin_amdgcn_mfma_f32_32x32x16_bf16(w1, h1, acc[1][1], 0, 0, 0);
    __builtin_amdgcn_sched_barrier(0);
    if constexpr (s + PD < 28) ldstep(s + PD);
    __builtin_amdgcn_sched_barrier(0);
  });
  u4 xr0 = *(const u4*)(x + oidx), xr1 = *(const u4*)(x + oidx + 8);
  float* red = (float*)smem;
  auto put = [&](int a, int b) {
#pragma unroll
    for (int g = 0; g < 4; ++g) {
      f4 v = {acc[a][b][4 * g], acc[a][b][4 * g + 1], acc[a][b][4 * g + 2], acc[a][b][4 * g + 3]};
      *(f4*)(red + (w * 64 + b * 32 + r32) * 68 + a * 32 + 8 * g + 4 * hh) = v;
    }
  };
  STAMP(2);
#pragma unroll
  for (int a = 0; a < 2; ++a)
#pragma unroll
    for (int b = 0; b < 2; ++b)
      if (w != 2 * a + b) put(a, b);
  {
    BAR();
    STAMP(3);
    const int dt = w >> 1, tt = w & 1;
    const int yo = ((nt * 64 + dt * 32 + r32) * 2048 + mt * 64 + tt * 32 + 8 * hh) * 2;
    u4 y0, y1;
    for (unsigned spins = 0; spins < 4000000u; ++spins) {
      y0 = __builtin_amdgcn_raw_buffer_load_b128(yrs, yo, 0, 16);
      y1 = __builtin_amdgcn_raw_buffer_load_b128(yrs, yo + 32, 0, 16);
      bool bad = false;
#pragma unroll
      for (int p = 0; p < 4; ++p) bad = bad || y0[p] == 0xffffffffu || y1[p] == 0xffffffffu;
      if (__builtin_amdgcn_ballot_w64(bad) == 0ull) break;
      __builtin_amdgcn_s_sleep(2);
      asm volatile("" ::: "memory");
    }
    u4 i0, i1;
#pragma unroll
    for (int p = 0; p < 4; ++p) {
      int k = 8 * hh + 2 * p;
      i0[p] = (k == r32 ? 0x3f80u : 0u) | (k + 1 == r32 ? 0x3f800000u : 0u);
      i1[p] = (k + 16 == r32 ? 0x3f80u : 0u) | (k + 17 == r32 ? 0x3f800000u : 0u);
    }
    f16v t;
#pragma unroll
    for (int e = 0; e < 16; ++e) t[e] = 0.f;
    t = __builtin_amdgcn_mfma_f32_32x32x16_bf16(__builtin_bit_cast(bf8, y0), __builtin_bit_cast(bf8, i0), t, 0, 0, 0);
    t = __builtin_amdgcn_mfma_f32_32x32x16_bf16(__builtin_bit_cast(bf8, y1), __builtin_bit_cast(bf8, i1), t, 0, 0, 0);
#pragma unroll
    for (int a = 0; a < 2; ++a)
#pragma unroll
      for (int b = 0; b < 2; ++b)
        if (w == 2 * a + b) { acc[a][b] += t; put(a, b); }
  }
  STAMP(4); WAITLGKM(); BAR(); STAMP(5);
  u4 o[2];
#pragma unroll
  for (int q = 0; q < 4; ++q) {
    const float* rp = red + tok * 68 + dc + 4 * q;
    f4 v = (*(const f4*)rp + *(const f4*)(rp + 64 * 68)) + (*(const f4*)(rp + 128 * 68) + *(const f4*)(rp + 192 * 68));
    unsigned xa = q < 2 ? xr0[2 * q] : xr1[2 * q - 4], xb = q < 2 ? xr0[2 * q + 1] : xr1[2 * q - 3];
    o[q >> 1][2 * (q & 1)] = pk_bf(v[0] + bf_lo(xa), v[1] + bf_hi(xa));
    o[q >> 1][2 * (q & 1) + 1] = pk_bf(v[2] + bf_lo(xb), v[3] + bf_hi(xb));
  }
  auto ors = __builtin_amdgcn_make_buffer_rsrc((void*)out, (short)0, 2048 * 512 * 2, 0x00020000);
  __builtin_amdgcn_raw_buffer_store_b128(o[0], ors, (unsigned)oidx * 2, 0, A_O);
  __builtin_amdgcn_raw_buffer_store_b128(o[1], ors, (unsigned)oidx * 2 + 16, 0, A_O);
  STAMP(6); STAMP_OUT(1);
}

extern "C" void kernel_launch(void* const* d_in, const int* in_sizes, int n_in, void* d_out, int out_size, void* d_ws,
                              size_t ws_size, hipStream_t stream) {
  (void)in_sizes; (void)n_in; (void)out_size; (void)ws_size;
  typedef const unsigned short* cp;
  (void)hipFuncSetAttribute((const void*)k1_kernel, hipFuncAttributeMaxDynamicSharedMemorySize, K1_LDS);
  (void)hipFuncSetAttribute((const void*)k2_kernel, hipFuncAttributeMaxDynamicSharedMemorySize, K2_LDS);
  k1_kernel<<<256, 256, K1_LDS, stream>>>((cp)d_in[0], (cp)d_in[8], (cp)d_in[9], (cp)d_in[10], (char*)d_ws, (cp)d_in[1], (cp)d_in[2],
                                          (cp)d_in[3], (cp)d_in[4], (cp)d_in[5], (cp)d_in[6], (cp)d_out);
  k2_kernel<<<256, 512, K2_LDS, stream>>>((cp)d_in[0], (cp)d_in[1], (cp)d_in[2], (cp)d_in[3], (cp)d_in[4], (cp)d_in[5],
                                          (cp)d_in[6], (char*)d_ws, (unsigned short*)d_out);
}
```

```cpp
#include <hip/hip_runtime.h>
#include <hip/hip_bf16.h>
#include <stdint.h>

typedef __attribute__((ext_vector_type(2))) __bf16 bf2;
typedef __attribute__((ext_vector_type(8))) __bf16 bf8;
typedef __attribute__((ext_vector_type(2))) _Float16 h2;
typedef __attribute__((ext_vector_type(8))) _Float16 h8;
typedef __attribute__((ext_vector_type(2))) float f2;
typedef __attribute__((ext_vector_type(4))) float f4;
typedef __attribute__((ext_vector_type(16))) float f16v;
typedef __attribute__((ext_vector_type(2))) unsigned u2;
typedef __attribute__((ext_vector_type(4))) unsigned u4;
typedef __attribute__((ext_vector_type(4))) short s4;
#define LDSP __attribute__((address_space(3)))
#define DEVI __device__ __forceinline__

constexpr int SEQ = 2048, DM = 512, FF = 1792;
constexpr int XT_ROW = 4608;
constexpr size_t OFF_H = 0;
constexpr size_t OFF_WF = OFF_H + (size_t)64 * 112 * 1024;
constexpr size_t OFF_XT = OFF_WF + (size_t)16 * 112 * 1024;
constexpr size_t OFF_YA = OFF_XT + (size_t)512 * XT_ROW + 1024;
constexpr size_t OFF_CNT = OFF_YA + (size_t)512 * 2048 * 2;

DEVI float bf_lo(unsigned v) { return __builtin_bit_cast(float, v << 16); }
DEVI float bf_hi(unsigned v) { return __builtin_bit_cast(float, v & 0xffff0000u); }
DEVI unsigned pk_bf(float a, float b) { f2 f = {a, b}; return __builtin_bit_cast(unsigned, __builtin_convertvector(f, bf2)); }
DEVI unsigned pk_h(float a, float b) { f2 f = {a, b}; return __builtin_bit_cast(unsigned, __builtin_convertvector(f, h2)); }
DEVI float rbf(float a) { return bf_lo(pk_bf(a, 0.f)); }
DEVI float ldbf(const unsigned short* p) { return __builtin_bit_cast(float, ((unsigned)*p) << 16); }
DEVI float dot2(unsigned a, unsigned b, float c) {
  return __builtin_amdgcn_fdot2_f32_bf16(__builtin_bit_cast(bf2, a), __builtin_bit_cast(bf2, b), c, false);
}
DEVI void glds16(const void* g, unsigned lds_base) {
  unsigned sv;
  asm volatile("s_mov_b32 %0, m0\n\ts_mov_b32 m0, %2\n\ts_nop 0\n\tglobal_load_lds_dwordx4 %1, off\n\ts_mov_b32 m0, %0"
               : "=&s"(sv) : "v"(g), "s"(lds_base) : "memory");
}
DEVI void glds16s(unsigned voff, const void* sbase, unsigned lds_base) {
  asm volatile("s_mov_b32 m0, %2\n\ts_nop 0\n\tglobal_load_lds_dwordx4 %0, %1" :: "v"(voff), "s"(sbase), "s"(lds_base) : "memory", "m0");
}
#define WAITVM(n) asm volatile("s_waitcnt vmcnt(" #n ")" ::: "memory")
#define WAITLGKM() asm volatile("s_waitcnt lgkmcnt(0)" ::: "memory")
#define BAR() do { asm volatile("" ::: "memory"); __builtin_amdgcn_s_barrier(); asm volatile("" ::: "memory"); } while (0)
#define GLD(dst, voff, sptr, imm) asm volatile("global_load_dwordx4 %0, %1, %2 offset:" #imm : "=v"(dst) : "v"(voff), "s"(sptr))
#define GLDH(dst, voff, sptr) asm volatile("global_load_ushort %0, %1, %2" : "=v"(dst) : "v"(voff), "s"(sptr))
#define SB() __builtin_amdgcn_sched_barrier(0)
template <int V> struct ic { static constexpr int value = V; };
template <int I, int N, class F> DEVI void static_for(F&& f) { if constexpr (I < N) { f(ic<I>{}); static_for<I + 1, N>(f); } }
template <int N> DEVI void waitvm() { asm volatile("s_waitcnt vmcnt(%0)" ::"n"(N) : "memory"); }

#define STAMP(i)
#define STAMP_DECL
#define STAMP_OUT(k)
struct cf { float r, i; };
DEVI cf cmul(cf a, cf b) { cf c; c.r = a.r * b.r - a.i * b.i; c.i = a.r * b.i + a.i * b.r; return c; }
DEVI cf zpow(float a2, float rev, float t) {
  float e = __builtin_amdgcn_exp2f(a2 * t);
  float p = __builtin_amdgcn_fractf(rev * t);
  cf c; c.r = e * __builtin_amdgcn_cosf(p); c.i = e * __builtin_amdgcn_sinf(p); return c;
}
DEVI float wave_sum(float v) {
#pragma unroll
  for (int o = 32; o >= 1; o >>= 1) v += __shfl_xor(v, o);
  return v;
}

constexpr int A_S = 16, A_W = 16, A_H = 0, A_O = 0, A_X = 0;
constexpr int K1_LDS = 10 * 16384;

DEVI void kgen_wave(char* sa, char* sb, int lane, float a2, float rev, cf C) {
  char* wq = sa + lane * 4; char* w0 = wq + 4352; char* w1 = sb + lane * 4; char* w2 = w1 + 4352;
  float* tab = (float*)(sa + 64);
  const int rd = (lane & 15) * 272 + (lane >> 4) * 16;
  cf z1 = zpow(a2, rev, 1.f), z16 = zpow(a2, rev, 16.f), z256 = zpow(a2, rev, 256.f), z1024 = zpow(a2, rev, 1024.f);
  const cf z32 = cmul(z16, z16), z2 = cmul(z1, z1), z512 = cmul(z256, z256), z768 = cmul(z512, z256);
  cf wz[4]; wz[0] = z1024; wz[1] = cmul(z1024, z256); wz[2] = cmul(z1024, z512); wz[3] = cmul(z1024, z768);
  cf e = {1.f, 0.f}, o = z16, pe = C, po = cmul(C, z1);
  { cf p = cmul(cmul(C, z1024), z1024); *(unsigned*)(w2 + 8 * 272) = pk_h(p.r, p.i); }
#pragma unroll
  for (int r = 0; r < 8; ++r) {
    *(unsigned*)(wq + (2 * r) * 272) = pk_h(e.r, -e.i);
    *(unsigned*)(wq + (2 * r + 1) * 272) = pk_h(o.r, -o.i);
    *(unsigned*)(w0 + (2 * r) * 272) = pk_h(pe.r, pe.i);
    *(unsigned*)(w0 + (2 * r + 1) * 272) = pk_h(po.r, po.i);
    { cf p = cmul(pe, z256); *(unsigned*)(w1 + r * 272) = pk_h(p.r, p.i); }
    if ((r & 1) == 0) {
      cf p = cmul(pe, z512), q = cmul(pe, z768);
      *(unsigned*)(w1 + (8 + (r >> 1)) * 272) = pk_h(p.r, p.i); *(unsigned*)(w1 + (12 + (r >> 1)) * 272) = pk_h(q.r, q.i);
    }
    if ((r & 3) == 0) {
#pragma unroll
      for (int f = 0; f < 4; ++f) { cf p = cmul(pe, wz[f]); *(unsigned*)(w2 + (2 * f + (r >> 2)) * 272) = pk_h(p.r, p.i); }
    }
    if (r < 7) { e = cmul(e, z32); o = cmul(o, z32); pe = cmul(pe, z2); po = cmul(po, z2); }
  }
  f4 a0 = {0.f, 0.f, 0.f, 0.f}, a1 = a0, a2v = a0;
#pragma unroll
  for (int s = 0; s < 4; ++s) {
    const h8 qf = *(const h8*)(sa + rd + s * 64);
    a0 = __builtin_amdgcn_mfma_f32_16x16x32_f16(*(const h8*)(sa + 4352 + rd + s * 64), qf, a0, 0, 0, 0);
    a1 = __builtin_amdgcn_mfma_f32_16x16x32_f16(*(const h8*)(sb + rd + s * 64), qf, a1, 0, 0, 0);
    a2v = __builtin_amdgcn_mfma_f32_16x16x32_f16(*(const h8*)(sb + 4352 + rd + s * 64), qf, a2v, 0, 0, 0);
  }
  const int col = lane & 15, rq = lane >> 4;
  *(f4*)(tab + 4 * rq + 16 * col) = a0;
  *(f4*)(tab + ((rq < 2) ? (256 + 4 * rq + 8 * col) : (384 + 64 * (rq & 1) + 4 * col))) = a1;
  if (rq < 2) {
    *(f2*)(tab + 512 + 64 * rq + 2 * col) = f2{a2v[0], a2v[1]};
    *(f2*)(tab + 512 + 64 * rq + 32 + 2 * col) = f2{a2v[2], a2v[3]};
  } else if (lane == 32) {
    tab[640] = a2v[0];
  }
}

DEVI void kput(char* kc, int blk, const float* v) {
  unsigned E[5], O[5];
#pragma unroll
  for (int k = 0; k < 5; ++k) { E[k] = pk_h(v[2 * k], v[2 * k + 1]); O[k] = pk_h(v[2 * k + 1], v[2 * k + 2]); }
  *(u4*)(kc + 16 * blk) = u4{E[0], E[1], E[2], E[3]};
  *(u4*)(kc + 4160 + 16 * blk) = u4{O[0], O[1], O[2], O[3]};
  *(u4*)(kc + 2 * 4160 + 16 * blk) = u4{E[1], E[2], E[3], E[4]};
  *(u4*)(kc + 3 * 4160 + 16 * blk) = u4{O[1], O[2], O[3], O[4]};
}
DEVI void kexpand(const float* tA, const float* tB, int t, int par, char* kc) {
  float v[11];
  {
    const int m = 255 - t;
    float z = tA[383 + m] + tB[383 + m], a = tA[384 + m] + tB[384 + m], b = tA[385 + m] + tB[385 + m];
    v[0] = b; v[1] = b; v[2] = b; v[3] = (m & 1) ? b : a; v[4] = a; v[5] = a; v[6] = a; v[7] = a; v[8] = a; v[9] = a;
    v[10] = m == 128 ? z : a;
    kput(kc, t, v);
  }
  const int m = 127 - t;
  if (par == 0) {
    const int bs = 256 + 2 * m;
    float y = tA[bs - 2] + tB[bs - 2], z = tA[bs - 1] + tB[bs - 1], a = tA[bs] + tB[bs], b = tA[bs + 1] + tB[bs + 1], c = tA[bs + 2] + tB[bs + 2];
    v[0] = c; v[1] = c; v[2] = b; v[3] = b; v[4] = b; v[5] = a; v[6] = a; v[7] = a; v[8] = a;
    v[9] = m == 64 ? z : a; v[10] = m == 64 ? y : z;
  } else {
    const bool s0 = m < 32, s1 = m <= 32, nz = m != 0;
    const int bs = s0 ? 8 * m - 3 : 125 + 4 * m;
    float W[11];
#pragma unroll
    for (int i = 0; i < 11; ++i) W[i] = tA[bs + i] + tB[bs + i];
    v[0] = s0 ? W[10] : W[7]; v[1] = s0 ? W[9] : W[6]; v[2] = s0 ? W[8] : W[5]; v[3] = s0 ? W[7] : W[5];
    v[4] = s0 ? W[6] : W[5]; v[5] = s0 ? W[5] : W[4]; v[6] = s0 ? W[4] : W[3]; v[7] = W[3];
    v[8] = nz ? (s1 ? W[2] : W[3]) : 0.f; v[9] = nz ? (s1 ? W[1] : W[2]) : 0.f; v[10] = nz ? (s1 ? W[0] : W[1]) : 0.f;
  }
  kput(kc, 128 + t, v);
}

DEVI bf8 tr_frag(unsigned addr) {
  s4 a = __builtin_amdgcn_ds_read_tr16_b64_v4i16((LDSP s4*)(uintptr_t)addr);
  s4 b = __builtin_amdgcn_ds_read_tr16_b64_v4i16((LDSP s4*)(uintptr_t)(addr + 256));
  typedef __attribute__((ext_vector_type(8))) short s8;
  s8 r = __builtin_shufflevector(a, b, 0, 1, 2, 3, 4, 5, 6, 7);
  return __builtin_bit_cast(bf8, r);
}
DEVI float ln_inv(float var) { return 1.f / rbf(__builtin_sqrtf(rbf(rbf(fmaxf(var, 0.f)) + 1e-5f))); }

DEVI void warm(const void* a0, const void* a1, const void* a2, const void* a3, const void* b0, const void* b1, const void* b2,
               unsigned lds0) {
  const unsigned t = threadIdx.x, bid = blockIdx.x;
  const unsigned w = __builtin_amdgcn_readfirstlane(t >> 6);
  const uintptr_t x0 = (w & 1) ? (uintptr_t)a1 : (uintptr_t)a0, x1 = (w & 1) ? (uintptr_t)a3 : (uintptr_t)a2;
  const uintptr_t y0 = (w & 1) ? (uintptr_t)b1 : (uintptr_t)b0;
  const uintptr_t pa = ((w & 2) ? x1 : x0) + bid * 512u;
  const uintptr_t pb = (w & 2) ? (uintptr_t)b2 + (bid * 8192u + (w & 1) * 4096u) : y0 + ((bid * 4u) & ~15u);
  const uintptr_t q = (t & 32) ? pb : pa + (t & 31) * 16u;
  glds16((const void*)q, lds0 + 5 * 16384 + w * 1024);
}

constexpr int slot_of(int s) { return (s + 8) % 10; }

__global__ void __launch_bounds__(256, 1)
k1_kernel(const unsigned short* __restrict__ x, const unsigned short* __restrict__ wg, const unsigned short* __restrict__ wu,
          const unsigned short* __restrict__ wd, char* __restrict__ ws, const unsigned short* __restrict__ logA,
          const unsigned short* __restrict__ Aim, const unsigned short* __restrict__ Cre, const unsigned short* __restrict__ Cim,
          const unsigned short* __restrict__ logdt, const unsigned short* __restrict__ dskip, const unsigned short* out) {
  extern __shared__ __attribute__((aligned(16))) char smem[];
  const int tid = threadIdx.x, lane = tid & 63;
  const int w = __builtin_amdgcn_readfirstlane(tid >> 6);
  const int bid = blockIdx.x, xcd = bid & 7, idx = bid >> 3;
  const int mt = 4 * (xcd >> 1) + idx / 7, ft = 7 * (xcd & 1) + idx % 7;
  const unsigned lds0 = (unsigned)(uintptr_t)(LDSP char*)smem;
  const int r32 = lane & 31, hh = lane >> 5;
  auto wrs = __builtin_amdgcn_make_buffer_rsrc((void*)ws, (short)0, (int)OFF_CNT, 0x00020000);
  STAMP_DECL; STAMP(0);
  {
    const u4 m = {~0u, ~0u, ~0u, ~0u};
    __builtin_amdgcn_raw_buffer_store_b128(m, wrs, (unsigned)OFF_YA + bid * 8192 + tid * 16, 0, A_S);
    __builtin_amdgcn_raw_buffer_store_b128(m, wrs, (unsigned)OFF_YA + bid * 8192 + 4096 + tid * 16, 0, A_S);
  }

  if (idx >= 28) {
    const int sp = xcd * 4 + (idx - 28);
    {
      const unsigned short* xp = x + (size_t)(sp * 64 + lane) * DM + w * 128;
      u4 xv[16];
#pragma unroll
      for (int j = 0; j < 16; ++j) xv[j] = *(const u4*)(xp + 8 * j);
      float s0 = 0.f, s1 = 0.f, q0 = 0.f, q1 = 0.f;
#pragma unroll
      for (int j = 0; j < 16; ++j)
#pragma unroll
        for (int p = 0; p < 4; p += 2) {
          s0 = dot2(xv[j][p], 0x3f803f80u, s0); s1 = dot2(xv[j][p + 1], 0x3f803f80u, s1);
          q0 = dot2(xv[j][p], xv[j][p], q0); q1 = dot2(xv[j][p + 1], xv[j][p + 1], q1);
        }
      f2* st2 = (f2*)smem;
      st2[w * 64 + lane] = f2{s0 + s1, q0 + q1};
      STAMP(3);
      __syncthreads();
      const f2 tt = (st2[lane] + st2[64 + lane]) + (st2[128 + lane] + st2[192 + lane]);
      const float mean = tt[0] * (1.f / 512.f), mb = rbf(mean);
      const float inv = ln_inv(tt[1] * (1.f / 512.f) - mean * mean);
      const unsigned xo = (unsigned)OFF_XT + (w * 128 + (lane & 1)) * XT_ROW + 144 * sp + (lane >> 1) * 4, sel = (lane & 1) ? 0x03020706u : 0x05040100u;
#pragma unroll
      for (int j = 0; j < 16; ++j)
#pragma unroll
        for (int p = 0; p < 4; ++p) {
          unsigned d = pk_bf(bf_lo(xv[j][p]) - mb, bf_hi(xv[j][p]) - mb);
          unsigned n = pk_bf(bf_lo(d) * inv, bf_hi(d) * inv);
          unsigned hv = pk_h(bf_lo(n), bf_hi(n));
          unsigned ot = __builtin_amdgcn_update_dpp(0u, hv, 0xB1, 0xf, 0xf, false);
          __builtin_amdgcn_raw_buffer_store_b32(__builtin_amdgcn_perm(ot, hv, sel), wrs, xo, (8 * j + 2 * p) * XT_ROW, A_X);
        }
    }
    STAMP(1);
    warm(logA, Aim, Cre, Cim, logdt, dskip, out, lds0);
    const int fr0 = (sp * 4 + w) * 14;
#pragma unroll 1
    for (int b = 0; b < 2; ++b) {
      unsigned short v[7][8];
      const int fr = fr0 + b * 7, nt = fr / 112, ks = fr % 112;
      const unsigned short* p = wd + (size_t)(16 * ks + 4 * hh) * DM + 32 * nt + r32;
#pragma unroll
      for (int i = 0; i < 7; ++i)
#pragma unroll
        for (int j = 0; j < 8; ++j) v[i][j] = p[(16 * i + 8 * (j >> 2) + (j & 3)) * DM];
#pragma unroll
      for (int i = 0; i < 7; ++i) {
        u4 o;
#pragma unroll
        for (int j = 0; j < 4; ++j) o[j] = (unsigned)v[i][2 * j] | ((unsigned)v[i][2 * j + 1] << 16);
        __builtin_amdgcn_raw_buffer_store_b128(o, wrs, (unsigned)OFF_WF + (fr + i) * 1024 + lane * 16, 0, A_W);
      }
    }
    STAMP(2); STAMP_OUT(0);
    WAITVM(0);
    return;
  }

  const unsigned wvo = ((8 * w + ((lane >> 2) & 7)) * FF + ft * 128 + (lane >> 5) * 32 + (lane & 3) * 8) * 2;
  const unsigned xsl = lds0 + w * 4096;
  auto issue_piece = [&](int s, int q) {
    glds16s(wvo, (q & 2 ? wu : wg) + (size_t)s * 32 * FF + (q & 1) * 64, xsl + slot_of(s) * 16384 + q * 1024);
  };
  auto issue_slab = [&](int s) { issue_piece(s, 0); issue_piece(s, 1); issue_piece(s, 2); issue_piece(s, 3); };
  u4 xf[32];
  float mean, var;
  {
    unsigned xvo[4];
#pragma unroll
    for (int j = 0; j < 4; ++j) xvo[j] = (mt * 128 + w * 32 + 4 * j + (lane >> 4)) * 1024 + ((((lane & 15) ^ (lane >> 4)) ^ (4 * j)) * 16);
    auto issue_x = [&](int c) {
#pragma unroll
      for (int j = 0; j < 8; ++j) glds16s(xvo[j & 3], x + 128 * c + (j >> 2) * 8192, xsl + c * 32768 + (j >> 2) * 16384 + 1024 * (j & 3));
    };
    issue_x(0); issue_x(1); issue_x(2);
    unsigned xb0 = xsl + (r32 >> 4) * 16384 + (r32 & 15) * 256, xb1 = xb0 + 65536;
    asm volatile("" : "+v"(xb0), "+v"(xb1));
    float s0 = 0.f, s1 = 0.f, q0 = 0.f, q1 = 0.f;
    static_for<0, 4>([&](auto H) {
      constexpr int c = H.value;
      waitvm<16>();
#pragma unroll
      for (int s = 0; s < 8; ++s)
        xf[8 * c + s] = *(const LDSP u4*)(uintptr_t)(((c & 2) ? xb1 : xb0) + (((2 * s + hh) ^ (r32 & 15)) * 16) + (c & 1) * 32768);
      if constexpr (c == 0) issue_x(3);
      if constexpr (c == 1) { issue_slab(0); issue_slab(1); }
#pragma unroll
      for (int s = 8 * c; s < 8 * c + 8; ++s)
#pragma unroll
        for (int p = 0; p < 4; p += 2) {
          s0 = dot2(xf[s][p], 0x3f803f80u, s0); s1 = dot2(xf[s][p + 1], 0x3f803f80u, s1);
          q0 = dot2(xf[s][p], xf[s][p], q0); q1 = dot2(xf[s][p + 1], xf[s][p + 1], q1);
        }
      asm volatile("" ::"v"(s0), "v"(s1), "v"(q0), "v"(q1) : "memory");
      if constexpr (c == 2) { issue_slab(2); issue_slab(3); }
      if constexpr (c == 3) issue_slab(4);
    });
    STAMP(1);
    float t = s0 + s1, q = q0 + q1;
    t += __shfl_xor(t, 32); q += __shfl_xor(q, 32);
    mean = t * (1.f / 512.f);
    var = q * (1.f / 512.f) - mean * mean;
  }
  STAMP(2);
  const float mb = rbf(mean);
  auto center = [&](int s, int p) { xf[s][p] = pk_bf(bf_lo(xf[s][p]) - mb, bf_hi(xf[s][p]) - mb); };
#pragma unroll
  for (int p = 0; p < 8; ++p) center(p >> 2, p & 3);
  f16v acc[8];
#pragma unroll
  for (int c = 0; c < 8; ++c)
#pragma unroll
    for (int e = 0; e < 16; ++e) acc[c][e] = 0.f;
  unsigned tb[3];
  tb[0] = lds0 + hh * 4096 + ((lane & 15) >> 2) * 64 + ((lane >> 4) & 1) * 32 + (lane & 3) * 8;
  tb[1] = tb[0] + 65536; tb[2] = tb[0] + 131072;
  asm volatile("" : "+v"(tb[0]), "+v"(tb[1]), "+v"(tb[2]));
  auto wfrag = [&](int n, int c) {
    const int sl = slot_of(n >> 2), nG = n & 3;
    return tr_frag(tb[sl >> 2] + (sl & 3) * 16384 + (nG >> 1) * 8192 + (nG & 1) * 2048 + c * 512);
  };
  waitvm<16>();
  BAR();
  bf8 wf[3][4];
#pragma unroll
  for (int c = 0; c < 8; ++c) wf[c >> 2][c & 3] = wfrag(c >> 2, c & 3);
  STAMP(4);
  SB();
  static_for<0, 64>([&](auto GG) {
    constexpr int g = GG.value, i = g >> 2, G = g & 3;
    if constexpr (G == 1 && i < 15) {
      waitvm<(i <= 11 ? 12 : 4 * (14 - i))>();
      BAR();
    }
    __builtin_amdgcn_s_waitcnt(g < 63 ? 0xC87F : 0xC07F);
    SB();
    const bf8 xb = __builtin_bit_cast(bf8, xf[2 * i + (G >> 1)]);
#pragma unroll
    for (int c = 0; c < 4; ++c) {
      acc[(G & 1) * 4 + c] = __builtin_amdgcn_mfma_f32_32x32x16_bf16(wf[g % 3][c], xb, acc[(G & 1) * 4 + c], 0, 0, 0);
      if constexpr (g + 2 < 64) wf[(g + 2) % 3][c] = wfrag(g + 2, c);
      if constexpr (g >= 1 && (g - 1) / 4 + 5 <= 15) { if (c == 1) issue_piece((g - 1) / 4 + 5, (g - 1) & 3); }
      if constexpr (i < 15) { if ((c & 1) == 0) center(2 * i + 2 + (G >> 1), (G & 1) * 2 + (c >> 1)); }
      SB();
    }
  });

  STAMP(5);
  warm(logA, Aim, Cre, Cim, logdt, dskip, out, lds0);
  const float inv = ln_inv(var);
  const float c1 = -inv * 1.44269504088896f, c2 = inv * inv;
  const unsigned hdst = (unsigned)OFF_H + ((mt * 4 + w) * 112 + ft * 8) * 1024 + lane * 16;
#pragma unroll
  for (int c = 0; c < 4; ++c) {
    unsigned o[8];
#pragma unroll
    for (int p = 0; p < 8; ++p) {
      f2 g = f2{acc[c][2 * p], acc[c][2 * p + 1]};
      f2 u = f2{acc[4 + c][2 * p], acc[4 + c][2 * p + 1]};
      f2 t = g * c1;
      f2 ex = f2{__builtin_amdgcn_exp2f(t[0]), __builtin_amdgcn_exp2f(t[1])} + 1.f;
      f2 r = f2{__builtin_amdgcn_rcpf(ex[0]), __builtin_amdgcn_rcpf(ex[1])};
      f2 hv = (g * u) * (r * c2);
      o[p] = pk_bf(hv[0], hv[1]);
    }
    u4 a = {o[0], o[1], o[2], o[3]}, b = {o[4], o[5], o[6], o[7]};
    __builtin_amdgcn_raw_buffer_store_b128(a, wrs, hdst + (2 * c) * 1024, 0, A_H);
    __builtin_amdgcn_raw_buffer_store_b128(b, wrs, hdst + (2 * c + 1) * 1024, 0, A_H);
    SB();
  }
  STAMP(6); STAMP_OUT(0);
  WAITVM(0);
}

constexpr int K2_RED = 4 * 64 * 272;
constexpr int C_XS = 0, C_K = 7424, C_Y = 7424 + 4 * 4160, C_SZ = C_Y + 20480;
constexpr int K2_SYNC = K2_RED + 2 * C_SZ;
constexpr int K2_LDS = K2_SYNC + 64;

DEVI void pair_sync(LDSP unsigned* c, unsigned target, int lane) {
  asm volatile("s_waitcnt vmcnt(0) lgkmcnt(0)" ::: "memory");
  if (lane == 0) __hip_atomic_fetch_add(c, 1u, __ATOMIC_RELAXED, __HIP_MEMORY_SCOPE_WORKGROUP);
  while (__hip_atomic_load(c, __ATOMIC_RELAXED, __HIP_MEMORY_SCOPE_WORKGROUP) < target) __builtin_amdgcn_s_sleep(1);
  asm volatile("" ::: "memory");
}
DEVI h8 kfrag(const LDSP char* p, const LDSP char* p8, int off) {
  u2 a = *(const LDSP u2*)(p + off), b = *(const LDSP u2*)(p8 + off);
  u4 r = {a[0], a[1], b[0], b[1]};
  return __builtin_bit_cast(h8, r);
}
constexpr int xoff(int v) { return 32 * v + 16 * (v >> 2); }

template <int U0, int U1>
DEVI void conv_near(const LDSP char* xa, const LDSP char* kb, const LDSP char* kb8, f16v& a0, f16v& a1) {
  constexpr int D = 3;
  h8 xq[D + 2], kq[D];
#pragma unroll
  for (int i = 0; i < D + 2; ++i) xq[i] = *(const LDSP h8*)(xa + xoff(U0 + i - 64));
#pragma unroll
  for (int i = 0; i < D; ++i) kq[i] = kfrag(kb, kb8, 32 * (U0 + i));
  SB();
  static_for<U0, U1>([&](auto U) {
    constexpr int u = U.value, i = u - U0;
    a0 = __builtin_amdgcn_mfma_f32_32x32x16_f16(xq[i % (D + 2)], kq[i % D], a0, 0, 0, 0);
    if constexpr (u + D < U1) xq[i % (D + 2)] = *(const LDSP h8*)(xa + xoff(u + D + 2 - 64));
    SB();
    a1 = __builtin_amdgcn_mfma_f32_32x32x16_f16(xq[(i + 2) % (D + 2)], kq[i % D], a1, 0, 0, 0);
    if constexpr (u + D < U1) kq[i % D] = kfrag(kb, kb8, 32 * (u + D));
    SB();
  });
}

__global__ void __launch_bounds__(512, 2)
k2_kernel(const unsigned short* __restrict__ x, const unsigned short* __restrict__ logA, const unsigned short* __restrict__ Aim,
          const unsigned short* __restrict__ Cre, const unsigned short* __restrict__ Cim, const unsigned short* __restrict__ logdt,
          const unsigned short* __restrict__ dskip, char* __restrict__ ws,
          unsigned short* __restrict__ out) {
  extern __shared__ __attribute__((aligned(16))) char smem[];
  const int tid = threadIdx.x, lane = tid & 63;
  const int w = __builtin_amdgcn_readfirstlane(tid >> 6);
  const int bid = blockIdx.x, xcd = bid & 7, idx = bid >> 3;
  const unsigned lds0 = (unsigned)(uintptr_t)(LDSP char*)smem;
  const int r32 = lane & 31, hh = lane >> 5;
  LDSP unsigned* sy = (LDSP unsigned*)(uintptr_t)(lds0 + K2_SYNC);
  auto yrs = __builtin_amdgcn_make_buffer_rsrc((void*)(ws + OFF_YA), (short)0, 512 * 2048 * 2, 0x00020000);

  STAMP_DECL; STAMP(0);
  unsigned pla_ = 0, pai_ = 0, pcr_ = 0, pci_ = 0, qcr_ = 0, qci_ = 0, pld_ = 0, dsk_ = 0;
  if (w >= 4) {
    __builtin_amdgcn_s_setprio(3);
    const unsigned nc = (2 * bid + ((w - 4) >> 1)) * 2, n0 = nc * 128 + ((w & 1) * 64 + lane) * 2, n1 = n0 ^ 128u;
    GLDH(pla_, n0, logA); GLDH(pai_, n0, Aim); GLDH(pcr_, n0, Cre); GLDH(pci_, n0, Cim);
    GLDH(qcr_, n1, Cre); GLDH(qci_, n1, Cim); GLDH(pld_, nc, logdt); GLDH(dsk_, nc, dskip);
  }
  if (w == 4 && lane < 16) sy[lane] = 0u;
  WAITLGKM(); BAR();
  if (w >= 4) {
    const int cl = (w - 4) >> 1, wv = w & 1, ch = 2 * bid + cl;
    char* cb = smem + K2_RED + cl * C_SZ;
    const unsigned cl0 = lds0 + K2_RED + cl * C_SZ;
    {
      const char* xsrc = ws + OFF_XT + (size_t)ch * XT_ROW + lane * 16;
      if (wv == 0) { glds16(xsrc, cl0 + 2304); glds16(xsrc + 1024, cl0 + 3328); glds16(xsrc + 2048, cl0 + 4352); }
      else { glds16(xsrc + 3072, cl0 + 5376); glds16(xsrc + 4096, cl0 + 6400); glds16(xsrc + 2048, cl0 + 4352); }
      u4 z = {0u, 0u, 0u, 0u};
      if (wv == 0) {
        *(u4*)(cb + lane * 16) = z; *(u4*)(cb + 1024 + lane * 16) = z;
        if (lane < 16) *(u4*)(cb + 2048 + lane * 16) = z;
      }
    }
    asm volatile("s_waitcnt vmcnt(3)" : "+v"(pla_), "+v"(pai_), "+v"(pcr_), "+v"(pci_), "+v"(qcr_), "+v"(qci_), "+v"(pld_), "+v"(dsk_));
    const float pla = bf_lo(pla_), pai = bf_lo(pai_), pcr = bf_lo(pcr_), pci = bf_lo(pci_), qcr = bf_lo(qcr_), qci = bf_lo(qci_);
    const float pld = bf_lo(pld_), dsk = bf_lo(dsk_);
    float scale;
    {
      float dt = rbf(__expf(pld));
      float are = -rbf(__expf(pla));
      float a2 = (are * dt) * 1.44269504088896f, rev = (pai * dt) * 0.159154943091895f;
      scale = 2.f * dt * wave_sum(fabsf(pcr) + fabsf(pci) + fabsf(qcr) + fabsf(qci));
      float isc = scale > 0.f ? 2.f * dt / scale : 0.f;
      cf C = {pcr * isc, pci * isc};
      STAMP(5);
      kgen_wave(cb + C_Y + wv * 10240, cb + C_K + wv * 8320, lane, a2, rev, C);
      STAMP(6);
      pair_sync(sy + 6 + cl, 2u, lane);
      const float* tA = (const float*)(cb + C_Y + 64);
      kexpand(tA, tA + 2560, wv * 64 + lane, wv, cb + C_K);
      if (wv == 0 && lane < 16) *(u4*)(cb + C_K + (lane >> 2) * 4160 + 4096 + (lane & 3) * 16) = u4{0u, 0u, 0u, 0u};
    }
    pair_sync(sy + cl, 2u, lane);
    STAMP(1);
    const int cc = (3 - r32) & 3;
    const LDSP char* cbl = (const LDSP char*)(uintptr_t)cl0;
    const LDSP char* kb = cbl + C_K + cc * 4160 + 2 * (8 * hh - r32 - 1 - cc);
    const LDSP char* kb8 = kb + 8;
    asm volatile("" : "+v"(kb8));
    const LDSP char* xa = cbl + 144 * r32 + 16 * hh;
    float* yb = (float*)(cb + C_Y);
    f16v a0, a1;
#pragma unroll
    for (int e = 0; e < 16; ++e) { a0[e] = 0.f; a1[e] = 0.f; }
    if (wv == 0) {
      conv_near<82, 130>(xa, kb, kb8, a0, a1);
    } else {
      const LDSP char* xfa = cbl + 144 * (r32 >> 1) + 16 * hh + 64 * (r32 & 1);
      const LDSP char* xfb = xfa + 16 * (r32 & 1);
      constexpr int D = 4;
      h8 xq[D], kq[D];
#pragma unroll
      for (int i = 0; i < D; ++i) { xq[i] = *(const LDSP h8*)((((2 + i) & 2) ? xfb : xfa) + xoff(2 + i)); kq[i] = kfrag(kb, kb8, 32 * (2 + i)); }
      SB();
      static_for<2, 66>([&](auto U) {
        constexpr int u = U.value, i = (u - 2) % D;
        if constexpr (u & 1) a1 = __builtin_amdgcn_mfma_f32_32x32x16_f16(xq[i], kq[i], a1, 0, 0, 0);
        else a0 = __builtin_amdgcn_mfma_f32_32x32x16_f16(xq[i], kq[i], a0, 0, 0, 0);
        if constexpr (u + D < 66) { xq[i] = *(const LDSP h8*)((((u + D) & 2) ? xfb : xfa) + xoff(u + D)); kq[i] = kfrag(kb, kb8, 32 * (u + D)); }
        SB();
      });
#pragma unroll
      for (int e = 0; e < 16; ++e) { yb[4096 + 32 * ((e & 3) + 8 * (e >> 2) + 4 * hh) + r32] = a0[e] + a1[e]; a0[e] = 0.f; a1[e] = 0.f; }
      conv_near<66, 82>(xa, kb, kb8, a0, a1);
    }
#pragma unroll
    for (int e = 0; e < 16; ++e) {
      int a = (e & 3) + 8 * (e >> 2) + 4 * hh;
      yb[2048 * wv + 64 * a + r32] = a0[e];
      yb[2048 * wv + 64 * a + 32 + r32] = a1[e];
    }
    STAMP(2);
    pair_sync(sy + 2 + cl, 2u, lane);
    STAMP(3);
#pragma unroll
    for (int rnd = 0; rnd < 2; ++rnd) {
      const int l = 1024 * wv + 512 * rnd + 8 * lane;
      f4 p0 = *(const f4*)(yb + l), p1 = *(const f4*)(yb + l + 4);
      p0 += *(const f4*)(yb + 2048 + l); p1 += *(const f4*)(yb + 2048 + l + 4);
      if (wv) { p0 += *(const f4*)(yb + 3072 + l); p1 += *(const f4*)(yb + 3072 + l + 4); }
      h8 xv = *(const h8*)(cb + 2304 + 2 * l + 16 * (l >> 6));
      u4 o;
#pragma unroll
      for (int p = 0; p < 4; ++p) {
        float y0 = p < 2 ? p0[2 * p] : p1[2 * p - 4], y1 = p < 2 ? p0[2 * p + 1] : p1[2 * p - 3];
        unsigned yv = pk_bf(y0 * scale, y1 * scale);
        unsigned sv = pk_bf((float)xv[2 * p] * dsk, (float)xv[2 * p + 1] * dsk);
        o[p] = pk_bf(bf_lo(yv) + bf_lo(sv), bf_hi(yv) + bf_hi(sv));
        o[p] = o[p] == 0xffffffffu ? 0xfffeffffu : o[p];
      }
      __builtin_amdgcn_raw_buffer_store_b128(o, yrs, (ch * 2048 + l) * 2, 0, 16);
    }
    STAMP(4); STAMP_OUT(1);
    return;
  }

  const int mt = 4 * xcd + (idx >> 3), nt = idx & 7;
  const char* hp = ws + OFF_H + ((size_t)(2 * mt) * 112 + 28 * w) * 1024;
  const char* wp = ws + OFF_WF + ((size_t)(2 * nt) * 112 + 28 * w) * 1024;
  const unsigned vo = lane * 16;
  constexpr int PD = 7;
  u4 hb[PD][2], wb[PD][2];
  auto ldstep = [&](int s) {
    const int b = s % PD, hi = (s >> 2) * 4096;
    switch (s & 3) {
      case 0: GLD(hb[b][0], vo, hp + hi, 0); GLD(wb[b][0], vo, wp + hi, 0); GLD(hb[b][1], vo, hp + 112 * 1024 + hi, 0); GLD(wb[b][1], vo, wp + 112 * 1024 + hi, 0); break;
      case 1: GLD(hb[b][0], vo, hp + hi, 1024); GLD(wb[b][0], vo, wp + hi, 1024); GLD(hb[b][1], vo, hp + 112 * 1024 + hi, 1024); GLD(wb[b][1], vo, wp + 112 * 1024 + hi, 1024); break;
      case 2: GLD(hb[b][0], vo, hp + hi, 2048); GLD(wb[b][0], vo, wp + hi, 2048); GLD(hb[b][1], vo, hp + 112 * 1024 + hi, 2048); GLD(wb[b][1], vo, wp + 112 * 1024 + hi, 2048); break;
      default: GLD(hb[b][0], vo, hp + hi, 3072); GLD(wb[b][0], vo, wp + hi, 3072); GLD(hb[b][1], vo, hp + 112 * 1024 + hi, 3072); GLD(wb[b][1], vo, wp + 112 * 1024 + hi, 3072); break;
    }
  };
  __builtin_amdgcn_s_sleep(8);
#pragma unroll
  for (int s = 0; s < PD; ++s) ldstep(s);
  const int tok = tid >> 2, dc = (tid & 3) * 16;
  const size_t oidx = (size_t)(mt * 64 + tok) * DM + nt * 64 + dc;
  STAMP(1);
  f16v acc[2][2];
#pragma unroll
  for (int e = 0; e < 16; ++e) { acc[0][0][e] = 0.f; acc[0][1][e] = 0.f; acc[1][0][e] = 0.f; acc[1][1][e] = 0.f; }
  static_for<0, 28>([&](auto S) {
    constexpr int s = S.value, b = s % PD;
    constexpr int left = (28 - 1 - s) < (PD - 1) ? (28 - 1 - s) : (PD - 1);
    asm volatile("s_waitcnt vmcnt(%4)" : "+v"(hb[b][0]), "+v"(hb[b][1]), "+v"(wb[b][0]), "+v"(wb[b][1]) : "n"(4 * left));
    const bf8 h0 = __builtin_bit_cast(bf8, hb[b][0]), h1 = __builtin_bit_cast(bf8, hb[b][1]);
    const bf8 w0 = __builtin_bit_cast(bf8, wb[b][0]), w1 = __builtin_bit_cast(bf8, wb[b][1]);
    acc[0][0] = __builtin_amdgcn_mfma_f32_32x32x16_bf16(w0, h0, acc[0][0], 0, 0, 0);
    acc[0][1] = __builtin_amdgcn_mfma_f32_32x32x16_bf16(w0, h1, acc[0][1], 0, 0, 0);
    acc[1][0] = __builtin_amdgcn_mfma_f32_32x32x16_bf16(w1, h0, acc[1][0], 0, 0, 0);
    acc[1][1] = __builtin_amdgcn_mfma_f32_32x32x16_bf16(w1, h1, acc[1][1], 0, 0, 0);
    __builtin_amdgcn_sched_barrier(0);
    if constexpr (s + PD < 28) ldstep(s + PD);
    __builtin_amdgcn_sched_barrier(0);
  });
  u4 xr0 = *(const u4*)(x + oidx), xr1 = *(const u4*)(x + oidx + 8);
  float* red = (float*)smem;
  auto put = [&](int a, int b) {
#pragma unroll
    for (int g = 0; g < 4; ++g) {
      f4 v = {acc[a][b][4 * g], acc[a][b][4 * g + 1], acc[a][b][4 * g + 2], acc[a][b][4 * g + 3]};
      *(f4*)(red + (w * 64 + b * 32 + r32) * 68 + a * 32 + 8 * g + 4 * hh) = v;
    }
  };
  STAMP(2);
#pragma unroll
  for (int a = 0; a < 2; ++a)
#pragma unroll
    for (int b = 0; b < 2; ++b)
      if (w != 2 * a + b) put(a, b);
  {
    BAR();
    STAMP(3);
    const int dt = w >> 1, tt = w & 1;
    const int yo = ((nt * 64 + dt * 32 + r32) * 2048 + mt * 64 + tt * 32 + 8 * hh) * 2;
    u4 y0, y1;
    for (unsigned spins = 0; spins < 4000000u; ++spins) {
      y0 = __builtin_amdgcn_raw_buffer_load_b128(yrs, yo, 0, 16);
      y1 = __builtin_amdgcn_raw_buffer_load_b128(yrs, yo + 32, 0, 16);
      bool bad = false;
#pragma unroll
      for (int p = 0; p < 4; ++p) bad = bad || y0[p] == 0xffffffffu || y1[p] == 0xffffffffu;
      if (__builtin_amdgcn_ballot_w64(bad) == 0ull) break;
      __builtin_amdgcn_s_sleep(2);
      asm volatile("" ::: "memory");
    }
    u4 i0, i1;
#pragma unroll
    for (int p = 0; p < 4; ++p) {
      int k = 8 * hh + 2 * p;
      i0[p] = (k == r32 ? 0x3f80u : 0u) | (k + 1 == r32 ? 0x3f800000u : 0u);
      i1[p] = (k + 16 == r32 ? 0x3f80u : 0u) | (k + 17 == r32 ? 0x3f800000u : 0u);
    }
    f16v t;
#pragma unroll
    for (int e = 0; e < 16; ++e) t[e] = 0.f;
    t = __builtin_amdgcn_mfma_f32_32x32x16_bf16(__builtin_bit_cast(bf8, y0), __builtin_bit_cast(bf8, i0), t, 0, 0, 0);
    t = __builtin_amdgcn_mfma_f32_32x32x16_bf16(__builtin_bit_cast(bf8, y1), __builtin_bit_cast(bf8, i1), t, 0, 0, 0);
#pragma unroll
    for (int a = 0; a < 2; ++a)
#pragma unroll
      for (int b = 0; b < 2; ++b)
        if (w == 2 * a + b) { acc[a][b] += t; put(a, b); }
  }
  STAMP(4); WAITLGKM(); BAR(); STAMP(5);
  u4 o[2];
#pragma unroll
  for (int q = 0; q < 4; ++q) {
    const float* rp = red + tok * 68 + dc + 4 * q;
    f4 v = (*(const f4*)rp + *(const f4*)(rp + 64 * 68)) + (*(const f4*)(rp + 128 * 68) + *(const f4*)(rp + 192 * 68));
    unsigned xa = q < 2 ? xr0[2 * q] : xr1[2 * q - 4], xb = q < 2 ? xr0[2 * q + 1] : xr1[2 * q - 3];
    o[q >> 1][2 * (q & 1)] = pk_bf(v[0] + bf_lo(xa), v[1] + bf_hi(xa));
    o[q >> 1][2 * (q & 1) + 1] = pk_bf(v[2] + bf_lo(xb), v[3] + bf_hi(xb));
  }
  auto ors = __builtin_amdgcn_make_buffer_rsrc((void*)out, (short)0, 2048 * 512 * 2, 0x00020000);
  __builtin_amdgcn_raw_buffer_store_b128(o[0], ors, (unsigned)oidx * 2, 0, A_O);
  __builtin_amdgcn_raw_buffer_store_b128(o[1], ors, (unsigned)oidx * 2 + 16, 0, A_O);
  STAMP(6); STAMP_OUT(1);
}

extern "C" void kernel_launch(void* const* d_in, const int* in_sizes, int n_in, void* d_out, int out_size, void* d_ws,
                              size_t ws_size, hipStream_t stream) {
  (void)in_sizes; (void)n_in; (void)out_size; (void)ws_size;
  typedef const unsigned short* cp;
  (void)hipFuncSetAttribute((const void*)k1_kernel, hipFuncAttributeMaxDynamicSharedMemorySize, K1_LDS);
  (void)hipFuncSetAttribute((const void*)k2_kernel, hipFuncAttributeMaxDynamicSharedMemorySize, K2_LDS);
  k1_kernel<<<256, 256, K1_LDS, stream>>>((cp)d_in[0], (cp)d_in[8], (cp)d_in[9], (cp)d_in[10], (char*)d_ws, (cp)d_in[1], (cp)d_in[2],
                                          (cp)d_in[3], (cp)d_in[4], (cp)d_in[5], (cp)d_in[6], (cp)d_out);
  k2_kernel<<<256, 512, K2_LDS, stream>>>((cp)d_in[0], (cp)d_in[1], (cp)d_in[2], (cp)d_in[3], (cp)d_in[4], (cp)d_in[5],
                                          (cp)d_in[6], (char*)d_ws, (unsigned short*)d_out);
}
```
